```python
import torch

def _softmax(x, axis=-1):
    return torch.softmax(x, dim=axis)
def _gelu(x):
    return torch.nn.functional.gelu(x, approximate="tanh")
def _silu(x):
    return torch.nn.functional.silu(x)
def _sigmoid(x):
    return torch.sigmoid(x)
def _layernorm(x, axis=-1, eps=1e-5):
    m = x.mean(dim=axis, keepdim=True)
    v = x.var(dim=axis, keepdim=True, unbiased=False)
    return (x - m) / torch.sqrt(v + eps)
def _scatter_add(tgt, idx, src, axis=0):
    return tgt.clone().index_add_(axis, idx, src)
def _segment_sum(src, seg, n):
    out = torch.zeros((n,) + src.shape[1:], dtype=src.dtype)
    return out.index_add_(0, seg, src)
def _pad(x, pw):
    flat = tuple(v for pair in reversed(pw) for v in pair)
    return torch.nn.functional.pad(x, flat)

kernel_name = 'parallel_sum__softmax_full_mha__mixer_s_mlp__prompt'

def setup_inputs():

    _w = (lambda a: (lambda t: t.to(torch.bfloat16) if t.is_floating_point() else t)(torch.as_tensor(a)))
    def _A_setup():
        B, S, D = 1, 2048, 512
        return {
            "x": _w((lambda *s: torch.randn(*s, dtype=torch.bfloat16) * 0.05)(B, S, D)),
            "wqkv": _w((lambda *s: torch.randn(*s, dtype=torch.bfloat16) * 0.1)(D, 3 * D)),
            "wo": _w((lambda *s: torch.randn(*s, dtype=torch.bfloat16) * 0.05)(D, D)),
        }
    _A_in = _A_setup()

    def _B_setup():
        B, S, D, F = 1, 2048, 512, 1376
        return {
            "x":  _w((lambda *s: torch.randn(*s, dtype=torch.bfloat16) * 0.05)(B, S, D)),
            "w1": _w((lambda *s: torch.randn(*s, dtype=torch.bfloat16) * 3.2)(D, D) * (D ** -0.5)),
            "w2": _w((lambda *s: torch.randn(*s, dtype=torch.bfloat16) * 3.2)(D, D) * (D ** -0.5)),
            "w3": _w((lambda *s: torch.randn(*s, dtype=torch.bfloat16) * 3.2)(D, F) * (D ** -0.5)),
            "w4": _w((lambda *s: torch.randn(*s, dtype=torch.bfloat16) * 3.2)(F, D) * (F ** -0.5)),
        }
    _B_in = _B_setup()

    return {
        "A_x": _A_in["x"],
        "A_wqkv": _A_in["wqkv"],
        "A_wo": _A_in["wo"],
        "B_x": _B_in["x"],
        "B_w1": _B_in["w1"],
        "B_w2": _B_in["w2"],
        "B_w3": _B_in["w3"],
        "B_w4": _B_in["w4"],
    }

def reference(A_x, A_wqkv, A_wo, B_x, B_w1, B_w2, B_w3, B_w4):
    def _A_ref(x, wqkv, wo):
        B, S, D = x.shape
        H = 32; Dh = D // H
        qkv = (lambda x, *d: x.permute(*d))((x @ wqkv).reshape(B, S, 3, H, Dh), 2, 0, 3, 1, 4)
        q, k, v = qkv[0], qkv[1], qkv[2]
        scale = Dh ** -0.5
        scores = torch.einsum("bhqd,bhkd->bhqk", q, k) * scale
        p = _softmax(scores, axis=-1)
        o = torch.einsum("bhqk,bhkd->bhqd", p, v)
        return (lambda x, *d: x.permute(*d))(o, 0, 2, 1, 3).reshape(B, S, D) @ wo

    def _B_ref(x, w1, w2, w3, w4):
        B, S, D = x.shape
        h = _gelu(_layernorm(x, axis=-1) @ w1)
        c = D // 4
        g0 = _pad(h[:, :, 0*c:1*c], ((0, 0), (1, 0), (0, 0)))[:, :S, :]
        g1 = _pad(h[:, :, 1*c:2*c], ((0, 0), (0, 1), (0, 0)))[:, 1:, :]
        g2 = _pad(h[:, :, 2*c:3*c], ((0, 0), (2, 0), (0, 0)))[:, :S, :]
        g3 = _pad(h[:, :, 3*c:   ], ((0, 0), (0, 2), (0, 0)))[:, 2:, :]
        hs = torch.concatenate([g0, g1, g2, g3], axis=-1) @ w2
        x1 = x + hs
        h2 = _gelu(_layernorm(x1, axis=-1) @ w3) @ w4
        return x1 + h2

    x = A_x
    xn = _layernorm(x, axis=-1)
    return x + _A_ref(xn, A_wqkv, A_wo) + _B_ref(xn, B_w1, B_w2, B_w3, B_w4)
```

```cpp
#include <hip/hip_runtime.h>
#include <stdint.h>

typedef __bf16 bf16_t;
typedef __attribute__((ext_vector_type(8))) __bf16 bf16x8;
typedef __attribute__((ext_vector_type(2))) __bf16 bf16x2;
typedef __attribute__((ext_vector_type(4))) short s16x4;
typedef __attribute__((ext_vector_type(8))) short s16x8;
typedef __attribute__((ext_vector_type(2))) float f32x2;
typedef __attribute__((ext_vector_type(4))) float f32x4;
typedef __attribute__((ext_vector_type(16))) float f32x16;
typedef __attribute__((ext_vector_type(4))) uint32_t u32x4;
typedef __attribute__((ext_vector_type(2))) uint32_t u32x2;

#define AS3 __attribute__((address_space(3)))
#define DEVI __device__ __forceinline__

constexpr int SEQ = 2048, DM = 512, FF = 1376, NH = 32;
constexpr float LN_EPS = 1e-5f;
constexpr float LOG2E = 1.4426950408889634f;

constexpr size_t WS_Q = 0;
constexpr size_t WS_K = WS_Q + (size_t)NH * 64 * 1024;
constexpr size_t WS_V = WS_K + (size_t)NH * 64 * 1024;
constexpr size_t WS_H = WS_V + (size_t)NH * 64 * 2048;
constexpr int HTOK = SEQ + 4;
constexpr size_t WS_ST = WS_H + (size_t)32 * 2 * HTOK * 16;
constexpr size_t WS_X1 = WS_ST + (size_t)SEQ * 8;
constexpr size_t WS_X1S = WS_X1 + (size_t)32 * 2 * SEQ * 16;
constexpr size_t WS_G = WS_X1S + (size_t)8 * SEQ * 8;
constexpr size_t WS_O = WS_G + (size_t)86 * 2 * SEQ * 16;
constexpr size_t WS_END = WS_O + (size_t)32 * 2 * SEQ * 16;

DEVI uint32_t pk2(float a, float b) {
  f32x2 v = {a, b};
  bf16x2 r = __builtin_convertvector(v, bf16x2);
  return __builtin_bit_cast(uint32_t, r);
}
DEVI float bf_lo(uint32_t u) { return __builtin_bit_cast(float, u << 16); }
DEVI float bf_hi(uint32_t u) { return __builtin_bit_cast(float, u & 0xffff0000u); }
DEVI float dot2_ones(uint32_t u, float c) {
  bf16x2 ones = {(__bf16)1.0f, (__bf16)1.0f};
  return __builtin_amdgcn_fdot2_f32_bf16(__builtin_bit_cast(bf16x2, u), ones, c, false);
}
DEVI float dot2_self(uint32_t u, float c) {
  bf16x2 a = __builtin_bit_cast(bf16x2, u);
  return __builtin_amdgcn_fdot2_f32_bf16(a, a, c, false);
}
DEVI f32x16 mfma32(bf16x8 a, bf16x8 b, f32x16 c) {
  return __builtin_amdgcn_mfma_f32_32x32x16_bf16(a, b, c, 0, 0, 0);
}
DEVI uint32_t lds_addr(const char* p) { return (uint32_t)(uintptr_t)(const AS3 char*)p; }
DEVI void glds16(const void* sbase, uint32_t voff, uint32_t lds_dst) {
  uint32_t keep;
  asm volatile(
      "s_mov_b32 %0, m0\n\ts_mov_b32 m0, %3\n\ts_nop 0\n\tglobal_load_lds_dwordx4 %1, %2\n\ts_mov_b32 m0, %0"
      : "=&s"(keep)
      : "v"(voff), "s"(sbase), "s"(lds_dst));
}
#define WAIT_VM(n) asm volatile("s_waitcnt vmcnt(" #n ")" ::: "memory")
template <int N>
DEVI void wait_vm() {
  asm volatile("s_waitcnt vmcnt(%0)" ::"i"(N) : "memory");
}
DEVI void bar_sync() {
  asm volatile("s_waitcnt lgkmcnt(0)\n\ts_barrier" ::: "memory");
}
DEVI bf16x8 lds_b128(const char* p) { return *(const bf16x8*)p; }
DEVI bf16x8 lds_tr2(const char* p0, const char* p1) {
  s16x4 a = __builtin_amdgcn_ds_read_tr16_b64_v4i16((AS3 s16x4*)p0);
  s16x4 b = __builtin_amdgcn_ds_read_tr16_b64_v4i16((AS3 s16x4*)p1);
  s16x8 c = __builtin_shufflevector(a, b, 0, 1, 2, 3, 4, 5, 6, 7);
  return __builtin_bit_cast(bf16x8, c);
}
DEVI float gelu_tanh(float x) {
  const float c0 = -2.0f * 0.7978845608028654f * LOG2E;
  const float c1 = c0 * 0.044715f;
  float t = x * __builtin_fmaf(x * x, c1, c0);
  float e = __builtin_amdgcn_exp2f(t);
  return x * __builtin_amdgcn_rcpf(1.0f + e);
}
DEVI u32x4 pack8(const f32x16& a, int s) {
  u32x4 r;
  r[0] = pk2(a[8 * s + 0], a[8 * s + 1]);
  r[1] = pk2(a[8 * s + 2], a[8 * s + 3]);
  r[2] = pk2(a[8 * s + 4], a[8 * s + 5]);
  r[3] = pk2(a[8 * s + 6], a[8 * s + 7]);
  return r;
}
DEVI float u2f(uint32_t u) { return __builtin_bit_cast(float, u); }
DEVI uint32_t f2u(float f) { return __builtin_bit_cast(uint32_t, f); }
DEVI void swap32(float a, float b, float& ra, float& rb) {
  auto r = __builtin_amdgcn_permlane32_swap(f2u(a), f2u(b), false, false);
  uint32_t r0 = r[0], r1 = r[1];
  ra = u2f(r0);
  rb = u2f(r1);
}
DEVI float xor32(float v) {
  return __shfl_xor(v, 32, 64);
}

constexpr int K1_SLOT = 32768, K1_NSLOT = 4;
constexpr int K1_LDS = K1_SLOT * K1_NSLOT + 8192;

template <bool isV>
DEVI void k1_body(char* smem, const bf16_t* __restrict__ x, const bf16_t* __restrict__ wqkv,
                  const bf16_t* __restrict__ w1, char* __restrict__ ws, int mt, int nt) {
  const int tid = threadIdx.x, lane = tid & 63;
  const int wid = __builtin_amdgcn_readfirstlane(tid >> 6);
  const int wm = wid >> 1, wn = wid & 1;
  const int m0 = mt * 128;
  const bool isH = nt >= 12;
  const bf16_t* wsrc = isH ? (w1 + (nt - 12) * 128) : (wqkv + nt * 128);
  const uint32_t wstride = isH ? 1024u : 3072u;
  const char* xsrc = (const char*)(x + (size_t)m0 * DM);
  const uint32_t lds0 = lds_addr(smem);

  const int r8 = lane >> 3, c8 = lane & 7;
  uint32_t xo[4], wo_[4];
#pragma unroll
  for (int i = 0; i < 4; ++i) {
    int piece = wid * 4 + i;
    int row = piece * 8 + r8;
    xo[i] = row * 1024 + ((c8 ^ ((row >> 1) & 7)) << 4);
    int g = piece >> 3, kr = (piece & 7) * 8 + r8;
    wo_[i] = kr * wstride + g * 128 + ((c8 ^ (((kr >> 1) & 1) << 2)) << 4);
  }
  auto issue = [&](int s) {
    uint32_t dst = lds0 + (s % K1_NSLOT) * K1_SLOT + wid * 4096;
#pragma unroll
    for (int i = 0; i < 4; ++i) glds16(xsrc, xo[i] + s * 128, dst + i * 1024);
#pragma unroll
    for (int i = 0; i < 4; ++i) glds16(wsrc, wo_[i] + s * 64 * wstride, dst + 16384 + i * 1024);
  };

  const int hh = lane >> 5, l31 = lane & 31;
  const int xsw = hh ^ ((l31 >> 1) & 7);
  const int xrow = (wm * 64 + l31) * 128;
  const int g2 = lane >> 4, q = (lane >> 2) & 3, p = lane & 3;
  const int wbase0 = 16384 + wn * 8192 + (8 * hh + q) * 128 + (((g2 & 1) * 32 + 8 * p) ^ (64 * (q >> 1)));

  f32x16 acc[2][2] = {};
  float sx[2] = {0.f, 0.f}, sxx[2] = {0.f, 0.f}, cw[2] = {0.f, 0.f};

  issue(0);
  issue(1);
  issue(2);
#pragma unroll
  for (int s = 0; s < 8; ++s) {
    if (s <= 5) wait_vm<16>();
    else if (s == 6) wait_vm<8>();
    else wait_vm<0>();
    bar_sync();
    if (s + 3 < 8) issue(s + 3);
    const char* sl = smem + (s % K1_NSLOT) * K1_SLOT;
#pragma unroll
    for (int kk = 0; kk < 4; ++kk) {
      bf16x8 xf[2], wf[2];
#pragma unroll
      for (int t = 0; t < 2; ++t) xf[t] = lds_b128(sl + xrow + t * 4096 + ((xsw ^ (2 * kk)) << 4));
#pragma unroll
      for (int u = 0; u < 2; ++u) {
        const char* pw = sl + (wbase0 ^ (u * 64)) + kk * 2048;
        wf[u] = lds_tr2(pw, pw + 512);
      }
      if (isV) {
#pragma unroll
        for (int t = 0; t < 2; ++t)
#pragma unroll
          for (int u = 0; u < 2; ++u) acc[t][u] = mfma32(xf[t], wf[u], acc[t][u]);
      } else {
#pragma unroll
        for (int t = 0; t < 2; ++t)
#pragma unroll
          for (int u = 0; u < 2; ++u) acc[t][u] = mfma32(wf[u], xf[t], acc[t][u]);
      }
      if ((kk & 1) == wn) {
#pragma unroll
        for (int t = 0; t < 2; ++t) {
          u32x4 ux = __builtin_bit_cast(u32x4, xf[t]);
#pragma unroll
          for (int j = 0; j < 4; ++j) {
            sx[t] = dot2_ones(ux[j], sx[t]);
            sxx[t] = dot2_self(ux[j], sxx[t]);
          }
        }
      }
      if ((kk & 1) == wm) {
#pragma unroll
        for (int u = 0; u < 2; ++u) {
          u32x4 uw = __builtin_bit_cast(u32x4, wf[u]);
#pragma unroll
          for (int j = 0; j < 4; ++j) cw[u] = dot2_ones(uw[j], cw[u]);
        }
      }
    }
  }
  float* st = (float*)(smem + K1_SLOT * K1_NSLOT);
  float* s_sx = st;
  float* s_sxx = st + 512;
  float* s_cw = st + 1024;
  float* s_mean = st + 1536;
  float* s_rs = st + 1664;
  float* s_cs = st + 1792;
#pragma unroll
  for (int t = 0; t < 2; ++t) {
    s_sx[(wm * 64 + t * 32 + l31) * 4 + wn * 2 + hh] = sx[t];
    s_sxx[(wm * 64 + t * 32 + l31) * 4 + wn * 2 + hh] = sxx[t];
    s_cw[(wn * 64 + t * 32 + l31) * 4 + wm * 2 + hh] = cw[t];
  }
  __syncthreads();
  if (tid < 128) {
    f32x4 a = *(const f32x4*)(s_sx + tid * 4), c = *(const f32x4*)(s_sxx + tid * 4);
    float mean = ((a[0] + a[1]) + (a[2] + a[3])) * (1.0f / DM);
    float ex2 = ((c[0] + c[1]) + (c[2] + c[3])) * (1.0f / DM);
    float var = fmaxf(ex2 - mean * mean, 0.f);
    float rstd = 1.0f / sqrtf(var + LN_EPS);
    if (nt == 0) *(f32x2*)(ws + WS_ST + (size_t)(m0 + tid) * 8) = f32x2{mean, rstd};
    float sc = rstd;
    if (isH) sc = rstd / sqrtf(var * rstd * rstd + LN_EPS);
    if (nt < 4) sc = rstd * (0.25f * LOG2E);
    s_mean[tid] = mean;
    s_rs[tid] = sc;
  } else {
    f32x4 a = *(const f32x4*)(s_cw + (tid - 128) * 4);
    s_cs[tid - 128] = (a[0] + a[1]) + (a[2] + a[3]);
  }
  __syncthreads();

  if (isV) {
    const uint32_t ONE2 = 0x3f803f80u;
#pragma unroll
    for (int u = 0; u < 2; ++u) {
      const float cs = s_cs[wn * 64 + u * 32 + l31];
#pragma unroll
      for (int t = 0; t < 2; ++t) {
        f32x16 v;
#pragma unroll
        for (int g = 0; g < 4; ++g) {
          int tk = wm * 64 + t * 32 + 8 * g + 4 * hh;
          f32x4 mn = *(const f32x4*)(s_mean + tk), rs = *(const f32x4*)(s_rs + tk);
#pragma unroll
          for (int j = 0; j < 4; ++j) v[4 * g + j] = (acc[t][u][4 * g + j] - mn[j] * cs) * rs[j];
        }
        const int head = (nt - 8) * 8 + wn * 4 + u * 2 + (l31 >> 4);
        const int tile = mt * 4 + wm * 2 + t;
        char* dst = ws + WS_V + ((size_t)head * 64 + tile) * 2048 + (hh * 32 + (l31 & 15)) * 16;
#pragma unroll
        for (int s = 0; s < 2; ++s) {
          *(u32x4*)(dst + s * 1024) = pack8(v, s);
          *(u32x4*)(dst + s * 1024 + 256) = u32x4{ONE2, ONE2, ONE2, ONE2};
        }
      }
    }
  } else {
#pragma unroll
    for (int t = 0; t < 2; ++t) {
      const int tk = wm * 64 + t * 32 + l31;
      const float mn = s_mean[tk], rs = s_rs[tk];
#pragma unroll
      for (int u = 0; u < 2; ++u) {
        f32x16 v;
#pragma unroll
        for (int g = 0; g < 4; ++g) {
          f32x4 cs = *(const f32x4*)(s_cs + wn * 64 + u * 32 + 8 * g + 4 * hh);
#pragma unroll
          for (int j = 0; j < 4; ++j) v[4 * g + j] = (acc[t][u][4 * g + j] - mn * cs[j]) * rs;
        }
        if (isH) {
#pragma unroll
          for (int r = 0; r < 16; ++r) v[r] = gelu_tanh(v[r]);
          const int k16 = (nt - 12) * 8 + wn * 4 + u * 2;
#pragma unroll
          for (int s = 0; s < 2; ++s)
            *(u32x4*)(ws + WS_H + (((size_t)(k16 + s) * 2 + hh) * HTOK + (m0 + tk + 2)) * 16) = pack8(v, s);
        } else {
          const int head = (nt & 3) * 8 + wn * 4 + u * 2;
          const int tile = mt * 4 + wm * 2 + t;
          char* base = ws + (nt < 4 ? WS_Q : WS_K);
#pragma unroll
          for (int s = 0; s < 2; ++s)
            *(u32x4*)(base + ((size_t)(head + s) * 64 + tile) * 1024 + lane * 16) = pack8(v, s);
        }
      }
    }
    if (isH && (mt == 0 || mt == 15) && tid < 32) {
      int k16 = (nt - 12) * 8 + (tid >> 2), h2 = (tid >> 1) & 1, pt = tid & 1;
      int tok = (mt == 0) ? pt : (SEQ + 2 + pt);
      *(u32x4*)(ws + WS_H + (((size_t)k16 * 2 + h2) * HTOK + tok) * 16) = u32x4{0, 0, 0, 0};
    }
  }
}

__global__ __launch_bounds__(256, 1) void k1_kernel(const bf16_t* __restrict__ x, const bf16_t* __restrict__ wqkv,
                                                    const bf16_t* __restrict__ w1, char* __restrict__ ws) {
  extern __shared__ __attribute__((aligned(16))) char smem[];
  const int b = blockIdx.x, xcd = b & 7, idx = b >> 3;
  const int mt = (xcd >> 1) * 4 + (idx & 3), nt = (xcd & 1) * 8 + (idx >> 2);
  if (nt >= 8 && nt < 12) k1_body<true>(smem, x, wqkv, w1, ws, mt, nt);
  else k1_body<false>(smem, x, wqkv, w1, ws, mt, nt);
}

constexpr int AT_SLOT = 24576, AT_NSLOT = 3;
constexpr int AT_LDS = AT_SLOT * AT_NSLOT;

DEVI void attention_block(char* smem, const char* __restrict__ ws_c, char* __restrict__ ws, int head, int qb) {
  const int tid = threadIdx.x, lane = tid & 63;
  const int wid = __builtin_amdgcn_readfirstlane(tid >> 6);
  const uint32_t lds0 = lds_addr(smem);
  const char* kbase = ws_c + WS_K + (size_t)head * 64 * 1024;
  const char* vbase = ws_c + WS_V + (size_t)head * 64 * 2048;
  const uint32_t lo = lane * 16;
  auto issue = [&](int c) {
    uint32_t dst = lds0 + (c % AT_NSLOT) * AT_SLOT;
    glds16(kbase, lo + c * 8192 + wid * 1024, dst + wid * 1024);
    glds16(vbase, lo + c * 16384 + wid * 2048, dst + 8192 + wid * 2048);
    glds16(vbase, lo + c * 16384 + wid * 2048 + 1024, dst + 8192 + wid * 2048 + 1024);
  };
  issue(0);
  issue(1);
  const int qtile = qb * 8 + wid;
  const bf16x8 qf = *(const bf16x8*)(ws_c + WS_Q + ((size_t)head * 64 + qtile) * 1024 + lane * 16);
  f32x16 o = {};
  f32x16 negm;
#pragma unroll
  for (int c = 0; c < 8; ++c) {
    if (c < 7) wait_vm<3>();
    else wait_vm<0>();
    bar_sync();
    if (c + 2 < 8) issue(c + 2);
    const char* sl = smem + (c % AT_NSLOT) * AT_SLOT + lane * 16;
    if (c == 0) {
      f32x16 z = {};
      f32x16 s0 = mfma32(lds_b128(sl), qf, z);
      float m = s0[0];
#pragma unroll
      for (int r = 1; r < 16; ++r) m = fmaxf(m, s0[r]);
      m = fmaxf(m, xor32(m));
#pragma unroll
      for (int r = 0; r < 16; ++r) negm[r] = -m;
    }
#pragma unroll
    for (int t = 0; t < 8; ++t) {
      f32x16 s = mfma32(lds_b128(sl + t * 1024), qf, negm);
      f32x16 pe;
#pragma unroll
      for (int r = 0; r < 16; ++r) pe[r] = __builtin_amdgcn_exp2f(s[r]);
      bf16x8 p0 = __builtin_bit_cast(bf16x8, pack8(pe, 0));
      bf16x8 p1 = __builtin_bit_cast(bf16x8, pack8(pe, 1));
      o = mfma32(lds_b128(sl + 8192 + t * 2048), p0, o);
      o = mfma32(lds_b128(sl + 8192 + t * 2048 + 1024), p1, o);
    }
  }
  float l = o[8];
  bool bad = !(l < 1.0e30f);
#pragma unroll
  for (int r = 0; r < 8; ++r) bad = bad || !(fabsf(o[r]) < 3.0e38f);
  if (__any(bad)) {
    f32x16 z = {};
    float m = -negm[0];
    o = z;
    for (int t = 0; t < 64; ++t) {
      bf16x8 kf = *(const bf16x8*)(kbase + (size_t)t * 1024 + lo);
      bf16x8 v0 = *(const bf16x8*)(vbase + (size_t)t * 2048 + lo);
      bf16x8 v1 = *(const bf16x8*)(vbase + (size_t)t * 2048 + 1024 + lo);
      f32x16 s = mfma32(kf, qf, z);
      float tm = s[0];
#pragma unroll
      for (int r = 1; r < 16; ++r) tm = fmaxf(tm, s[r]);
      tm = fmaxf(tm, xor32(tm));
      float mn = fmaxf(m, tm);
      float al = __builtin_amdgcn_exp2f(m - mn);
      m = mn;
      f32x16 pe;
#pragma unroll
      for (int r = 0; r < 16; ++r) {
        pe[r] = __builtin_amdgcn_exp2f(s[r] - mn);
        o[r] *= al;
      }
      bf16x8 p0 = __builtin_bit_cast(bf16x8, pack8(pe, 0));
      bf16x8 p1 = __builtin_bit_cast(bf16x8, pack8(pe, 1));
      o = mfma32(v0, p0, o);
      o = mfma32(v1, p1, o);
    }
    l = o[8];
  }
  const float inv = 1.0f / l;
  f32x16 on;
#pragma unroll
  for (int r = 0; r < 8; ++r) on[r] = o[r] * inv;
  const int tok = qtile * 32 + (lane & 31);
  *(u32x4*)(ws + WS_O + (((size_t)head * 2 + (lane >> 5)) * SEQ + tok) * 16) = pack8(on, 0);
}

__global__ __launch_bounds__(512, 2) void k2a_kernel(char* __restrict__ ws) {
  extern __shared__ __attribute__((aligned(16))) char smem[];
  const int b = blockIdx.x, xcd = b & 7, idx = b >> 3;
  attention_block(smem, ws, ws, xcd * 4 + (idx >> 3), idx & 7);
}

constexpr int W2_SLOT = 16384, W2_NSLOT = 4;
constexpr int W2_LDS = W2_SLOT * W2_NSLOT + 4096;

DEVI void w2_stage(char* smem, const bf16_t* __restrict__ x, const bf16_t* __restrict__ w2, char* __restrict__ ws,
                   int tg  , int nb  ) {
  const int tid = threadIdx.x, lane = tid & 63;
  const int wid = __builtin_amdgcn_readfirstlane(tid >> 6);
  const int wm = wid >> 1, wn = wid & 1;
  const int t0 = tg * 64;
  const uint32_t lds0 = lds_addr(smem);
  const char* hsrc = ws + WS_H;
  const char* wsrc = (const char*)(w2 + nb * 64);
  const int r8 = lane >> 3, c8 = lane & 7;
  uint32_t wo_[2];
#pragma unroll
  for (int i = 0; i < 2; ++i) {
    int kr = (wid * 2 + i) * 8 + r8;
    wo_[i] = kr * 1024 + ((c8 ^ (((kr >> 1) & 1) << 2)) << 4);
  }
  auto issue = [&](int s) {
    uint32_t dst = lds0 + (s % W2_NSLOT) * W2_SLOT;
    const int grp = s >> 1;
    const int sh = (grp == 0) ? -1 : (grp == 1) ? 1 : (grp == 2) ? -2 : 2;
#pragma unroll
    for (int h2 = 0; h2 < 2; ++h2)
      glds16(hsrc, (uint32_t)(((s * 4 + wid) * 2 + h2) * HTOK + t0 + 2 + sh + lane) * 16, dst + (wid * 2 + h2) * 1024);
#pragma unroll
    for (int i = 0; i < 2; ++i) glds16(wsrc, wo_[i] + s * 65536, dst + 8192 + (wid * 2 + i) * 1024);
  };
  const int hh = lane >> 5, l31 = lane & 31;
  const int g2 = lane >> 4, q = (lane >> 2) & 3, p = lane & 3;
  const int xoff = hh * 1024 + (wm * 32 + l31) * 16;
  const int woff = 8192 + (4 * hh + q) * 128 + ((wn * 64 + (g2 & 1) * 32 + 8 * p) ^ (64 * (q >> 1)));
  f32x16 acc = {};
  issue(0);
  issue(1);
  issue(2);
  const int tok = t0 + wm * 32 + l31;
  u32x4 xr[2];
#pragma unroll
  for (int s = 0; s < 2; ++s)
    xr[s] = *(const u32x4*)(x + (size_t)tok * DM + nb * 64 + wn * 32 + s * 16 + hh * 8);
  const f32x2 stt = *(const f32x2*)(ws + WS_ST + (size_t)tok * 8);
#pragma unroll
  for (int s = 0; s < 8; ++s) {
    if (s <= 5) wait_vm<8>();
    else if (s == 6) wait_vm<4>();
    else wait_vm<0>();
    bar_sync();
    if (s + 3 < 8) issue(s + 3);
    const char* sl = smem + (s % W2_NSLOT) * W2_SLOT;
#pragma unroll
    for (int kk = 0; kk < 4; ++kk) {
      bf16x8 xf = lds_b128(sl + kk * 2048 + xoff);
      bf16x8 wf = lds_tr2(sl + woff + kk * 2048, sl + woff + kk * 2048 + 1024);
      acc = mfma32(wf, xf, acc);
    }
  }
  f32x16 v;
  float s1 = 0.f, s2 = 0.f;
#pragma unroll
  for (int s = 0; s < 2; ++s) {
    auto a = __builtin_amdgcn_permlane32_swap(xr[s][0], xr[s][2], false, false);
    auto c = __builtin_amdgcn_permlane32_swap(xr[s][1], xr[s][3], false, false);
    uint32_t d[4] = {a[0], c[0], a[1], c[1]};
#pragma unroll
    for (int j = 0; j < 4; ++j) {
      v[8 * s + 2 * j] = (bf_lo(d[j]) - stt[0]) * stt[1] + acc[8 * s + 2 * j];
      v[8 * s + 2 * j + 1] = (bf_hi(d[j]) - stt[0]) * stt[1] + acc[8 * s + 2 * j + 1];
    }
  }
#pragma unroll
  for (int s = 0; s < 2; ++s) {
    u32x4 pk = pack8(v, s);
    *(u32x4*)(ws + WS_X1 + (((size_t)(nb * 4 + wn * 2 + s) * 2 + hh) * SEQ + tok) * 16) = pk;
#pragma unroll
    for (int j = 0; j < 4; ++j) {
      s1 = dot2_ones(pk[j], s1);
      s2 = dot2_self(pk[j], s2);
    }
  }
  s1 += xor32(s1);
  s2 += xor32(s2);
  float* red = (float*)(smem + W2_SLOT * W2_NSLOT);
  if (hh == 0) *(f32x2*)(red + (wn * 64 + wm * 32 + l31) * 2) = f32x2{s1, s2};
  __syncthreads();
  if (tid < 64) {
    f32x2 a = *(const f32x2*)(red + tid * 2), c = *(const f32x2*)(red + (64 + tid) * 2);
    *(f32x2*)(ws + WS_X1S + ((size_t)nb * SEQ + t0 + tid) * 8) = f32x2{a[0] + c[0], a[1] + c[1]};
  }
}

__global__ __launch_bounds__(256, 2) void k2b_kernel(const bf16_t* __restrict__ x, const bf16_t* __restrict__ w2,
                                                     char* __restrict__ ws) {
  extern __shared__ __attribute__((aligned(16))) char smem[];
  const int b = blockIdx.x, xcd = b & 7, idx = b >> 3;
  w2_stage(smem, x, w2, ws, xcd * 4 + (idx >> 3), idx & 7);
}

constexpr int W3_SLOT = 16384, W3_NSLOT = 4;
constexpr int W3_LDS = W3_SLOT * W3_NSLOT + 4096;

DEVI void w3_stage(char* smem, const bf16_t* __restrict__ w3, char* __restrict__ ws, int tg, int nb) {
  const int tid = threadIdx.x, lane = tid & 63;
  const int wid = __builtin_amdgcn_readfirstlane(tid >> 6);
  const int wm = wid >> 1, wn = wid & 1;
  const int t0 = tg * 64, n0 = nb * 192;
  const uint32_t lds0 = lds_addr(smem);
  const char* xsrc = ws + WS_X1;
  const char* wsrc = (const char*)w3;
  const int r8 = lane >> 3, c8 = lane & 7;
  uint32_t wo_[3];
#pragma unroll
  for (int i = 0; i < 3; ++i) {
    int piece = wid * 3 + i, g = piece >> 2, kr = (piece & 3) * 8 + r8;
    int col = n0 + g * 64 + ((c8 ^ (((kr >> 1) & 1) << 2)) << 3);
    col = min(col, FF - 8);
    wo_[i] = kr * (FF * 2) + col * 2;
  }
  auto issue = [&](int s) {
    uint32_t dst = lds0 + (s % W3_NSLOT) * W3_SLOT;
    glds16(xsrc, (uint32_t)((s * 4 + wid) * SEQ + t0 + lane) * 16, dst + wid * 1024);
#pragma unroll
    for (int i = 0; i < 3; ++i) glds16(wsrc, wo_[i] + s * 32 * (FF * 2), dst + 4096 + (wid * 3 + i) * 1024);
  };
  const int hh = lane >> 5, l31 = lane & 31;
  const int g2 = lane >> 4, q = (lane >> 2) & 3, p = lane & 3;
  const int xoff = hh * 1024 + (wm * 32 + l31) * 16;
  int woff[3];
#pragma unroll
  for (int u = 0; u < 3; ++u) {
    int n32 = wn * 3 + u;
    woff[u] = 4096 + (n32 >> 1) * 4096 + (4 * hh + q) * 128 + (((n32 & 1) * 64 + (g2 & 1) * 32 + 8 * p) ^ (64 * (q >> 1)));
  }
  f32x16 acc[3] = {};
  float cw[3] = {0.f, 0.f, 0.f};
  issue(0);
  issue(1);
  issue(2);
  const int tok = t0 + wm * 32 + l31;
  float s1 = 0.f, s2 = 0.f;
#pragma unroll
  for (int i = 0; i < 8; ++i) {
    f32x2 a = *(const f32x2*)(ws + WS_X1S + ((size_t)i * SEQ + tok) * 8);
    s1 += a[0];
    s2 += a[1];
  }
#pragma unroll 1
  for (int s4 = 0; s4 < 4; ++s4) {
#pragma unroll
    for (int si = 0; si < 4; ++si) {
      const int s = s4 * 4 + si;
      if (s4 < 3 || si < 2) wait_vm<8>();
      else if (si == 2) wait_vm<4>();
      else wait_vm<0>();
      bar_sync();
      if (s4 < 3 || si < 1) issue(s + 3);
      const char* sl = smem + si * W3_SLOT;
#pragma unroll
      for (int kk = 0; kk < 2; ++kk) {
        bf16x8 xf = lds_b128(sl + kk * 2048 + xoff);
#pragma unroll
        for (int u = 0; u < 3; ++u) {
          bf16x8 wf = lds_tr2(sl + woff[u] + kk * 2048, sl + woff[u] + kk * 2048 + 1024);
          acc[u] = mfma32(wf, xf, acc[u]);
          if (kk == wm) {
            u32x4 uw = __builtin_bit_cast(u32x4, wf);
#pragma unroll
            for (int j = 0; j < 4; ++j) cw[u] = dot2_ones(uw[j], cw[u]);
          }
        }
      }
    }
  }
  float* s_cw = (float*)(smem + W3_SLOT * W3_NSLOT);
  float* s_cs = s_cw + 768;
#pragma unroll
  for (int u = 0; u < 3; ++u) s_cw[(wn * 96 + u * 32 + l31) * 4 + wm * 2 + hh] = cw[u];
  __syncthreads();
  if (tid < 192) {
    f32x4 a = *(const f32x4*)(s_cw + tid * 4);
    s_cs[tid] = (a[0] + a[1]) + (a[2] + a[3]);
  }
  __syncthreads();
  const float mean = s1 * (1.0f / DM);
  const float var = fmaxf(s2 * (1.0f / DM) - mean * mean, 0.f);
  const float rs = 1.0f / sqrtf(var + LN_EPS);
#pragma unroll
  for (int u = 0; u < 3; ++u) {
    const int nn = n0 + wn * 96 + u * 32;
    if (nn < FF) {
      f32x16 v;
#pragma unroll
      for (int g = 0; g < 4; ++g) {
        f32x4 cs = *(const f32x4*)(s_cs + wn * 96 + u * 32 + 8 * g + 4 * hh);
#pragma unroll
        for (int j = 0; j < 4; ++j) v[4 * g + j] = gelu_tanh((acc[u][4 * g + j] - mean * cs[j]) * rs);
      }
#pragma unroll
      for (int s = 0; s < 2; ++s)
        *(u32x4*)(ws + WS_G + (((size_t)(nn / 16 + s) * 2 + hh) * SEQ + tok) * 16) = pack8(v, s);
    }
  }
}

__global__ __launch_bounds__(256, 2) void k2c_kernel(const bf16_t* __restrict__ w3, char* __restrict__ ws) {
  extern __shared__ __attribute__((aligned(16))) char smem[];
  const int b = blockIdx.x, xcd = b & 7, idx = b >> 3;
  w3_stage(smem, w3, ws, xcd * 4 + (idx >> 3), idx & 7);
}

constexpr int K3_SLOT = 16384, K3_NSLOT = 8;
constexpr int K3_LDS = K3_SLOT * K3_NSLOT;

__global__ __launch_bounds__(256, 1) void k3_kernel(const bf16_t* __restrict__ x, const bf16_t* __restrict__ wo,
                                                    const bf16_t* __restrict__ w4, const char* __restrict__ ws,
                                                    bf16_t* __restrict__ out) {
  extern __shared__ __attribute__((aligned(16))) char smem[];
  const int tid = threadIdx.x, lane = tid & 63;
  const int wid = __builtin_amdgcn_readfirstlane(tid >> 6);
  const int wm = wid >> 1, wn = wid & 1;
  const int b = blockIdx.x, xcd = b & 7, idx = b >> 3;
  const int tg = xcd * 4 + (idx >> 3), nb = idx & 7;
  const int t0 = tg * 64;
  const uint32_t lds0 = lds_addr(smem);
  const int r8 = lane >> 3, c8 = lane & 7;
  uint32_t wo_[2];
#pragma unroll
  for (int i = 0; i < 2; ++i) {
    int kr = (wid * 2 + i) * 8 + r8;
    wo_[i] = kr * 1024 + ((c8 ^ (((kr >> 1) & 1) << 2)) << 4) + nb * 128;
  }
  const uint32_t xo = (uint32_t)(t0 + lane) * 16;
  auto issue = [&](int s) {
    uint32_t dst = lds0 + (s % K3_NSLOT) * K3_SLOT;
    const bool first = s < 8;
    const int sl = first ? s : s - 8;
    const char* xs = ws + (first ? WS_O : WS_G);
    const char* wsb = (const char*)(first ? wo : w4);
    int k16 = sl * 4 + wid;
    int rowadj = 0;
    if (!first && k16 >= 86) { k16 -= 2; }
    if (!first && sl * 64 + wid * 16 >= FF) rowadj = -32;
#pragma unroll
    for (int h2 = 0; h2 < 2; ++h2)
      glds16(xs, xo + (uint32_t)(k16 * 2 + h2) * (SEQ * 16), dst + (wid * 2 + h2) * 1024);
#pragma unroll
    for (int i = 0; i < 2; ++i)
      glds16(wsb, wo_[i] + (uint32_t)(sl * 64 + rowadj) * 1024, dst + 8192 + (wid * 2 + i) * 1024);
  };
  const int hh = lane >> 5, l31 = lane & 31;
  const int g2 = lane >> 4, q = (lane >> 2) & 3, p = lane & 3;
  const int xoff = hh * 1024 + (wm * 32 + l31) * 16;
  const int woff = 8192 + (4 * hh + q) * 128 + ((wn * 64 + (g2 & 1) * 32 + 8 * p) ^ (64 * (q >> 1)));
  f32x16 acc = {};
#pragma unroll
  for (int s = 0; s < K3_NSLOT - 1; ++s) issue(s);
  const int tok = t0 + wm * 32 + l31;
  u32x4 xr[2], x1r[2];
#pragma unroll
  for (int s = 0; s < 2; ++s) {
    xr[s] = *(const u32x4*)(x + (size_t)tok * DM + nb * 64 + wn * 32 + s * 16 + hh * 8);
    x1r[s] = *(const u32x4*)(ws + WS_X1 + (((size_t)(nb * 4 + wn * 2 + s) * 2 + hh) * SEQ + tok) * 16);
  }
  constexpr int NS = 30;
  auto compute = [&](int slot, int nkk) {
    const char* sl = smem + slot * K3_SLOT;
#pragma unroll
    for (int kk = 0; kk < 4; ++kk) {
      if (kk < nkk) {
        bf16x8 xf = lds_b128(sl + kk * 2048 + xoff);
        bf16x8 wf = lds_tr2(sl + woff + kk * 2048, sl + woff + kk * 2048 + 1024);
        acc = mfma32(wf, xf, acc);
      }
    }
  };
#pragma unroll 1
  for (int s = 0; s <= NS - K3_NSLOT; ++s) {
    wait_vm<4 * (K3_NSLOT - 2)>();
    bar_sync();
    issue(s + K3_NSLOT - 1);
    compute(s % K3_NSLOT, 4);
  }
#pragma unroll
  for (int i = 0; i < K3_NSLOT - 1; ++i) {
    const int s = NS - K3_NSLOT + 1 + i;
    if (i == 0) wait_vm<24>();
    else if (i == 1) wait_vm<20>();
    else if (i == 2) wait_vm<16>();
    else if (i == 3) wait_vm<12>();
    else if (i == 4) wait_vm<8>();
    else if (i == 5) wait_vm<4>();
    else wait_vm<0>();
    bar_sync();
    compute(s % K3_NSLOT, s == NS - 1 ? 2 : 4);
  }
#pragma unroll
  for (int s = 0; s < 2; ++s) {
    float v[8];
#pragma unroll
    for (int j = 0; j < 4; ++j) {
      v[2 * j] = acc[8 * s + 2 * j] + bf_lo(x1r[s][j]);
      v[2 * j + 1] = acc[8 * s + 2 * j + 1] + bf_hi(x1r[s][j]);
    }
    float w[8];
#pragma unroll
    for (int j = 0; j < 4; ++j) {
      swap32(v[j], v[4 + j], w[j], w[4 + j]);
    }
    u32x4 o;
#pragma unroll
    for (int j = 0; j < 4; ++j) o[j] = pk2(w[2 * j] + bf_lo(xr[s][j]), w[2 * j + 1] + bf_hi(xr[s][j]));
    *(u32x4*)(out + (size_t)tok * DM + nb * 64 + wn * 32 + s * 16 + hh * 8) = o;
  }
}

extern "C" void kernel_launch(void* const* d_in, const int* in_sizes, int n_in, void* d_out, int out_size, void* d_ws,
                              size_t ws_size, hipStream_t stream) {
  const bf16_t* x = (const bf16_t*)d_in[0];
  const bf16_t* wqkv = (const bf16_t*)d_in[1];
  const bf16_t* wo = (const bf16_t*)d_in[2];
  const bf16_t* w1 = (const bf16_t*)d_in[4];
  const bf16_t* w2 = (const bf16_t*)d_in[5];
  const bf16_t* w3 = (const bf16_t*)d_in[6];
  const bf16_t* w4 = (const bf16_t*)d_in[7];
  char* ws = (char*)d_ws;
  (void)hipFuncSetAttribute((const void*)k1_kernel, hipFuncAttributeMaxDynamicSharedMemorySize, K1_LDS);
  (void)hipFuncSetAttribute((const void*)k2a_kernel, hipFuncAttributeMaxDynamicSharedMemorySize, AT_LDS);
  (void)hipFuncSetAttribute((const void*)k2b_kernel, hipFuncAttributeMaxDynamicSharedMemorySize, W2_LDS);
  (void)hipFuncSetAttribute((const void*)k2c_kernel, hipFuncAttributeMaxDynamicSharedMemorySize, W3_LDS);
  (void)hipFuncSetAttribute((const void*)k3_kernel, hipFuncAttributeMaxDynamicSharedMemorySize, K3_LDS);
  k1_kernel<<<256, 256, K1_LDS, stream>>>(x, wqkv, w1, ws);
  k2a_kernel<<<256, 512, AT_LDS, stream>>>(ws);
  k2b_kernel<<<256, 256, W2_LDS, stream>>>(x, w2, ws);
  k2c_kernel<<<256, 256, W3_LDS, stream>>>(w3, ws);
  k3_kernel<<<256, 256, K3_LDS, stream>>>(x, wo, w4, ws, (bf16_t*)d_out);
}
```

```cpp
#include <hip/hip_runtime.h>
#include <stdint.h>

typedef __bf16 bf16_t;
typedef __attribute__((ext_vector_type(8))) __bf16 bf16x8;
typedef __attribute__((ext_vector_type(2))) __bf16 bf16x2;
typedef __attribute__((ext_vector_type(4))) short s16x4;
typedef __attribute__((ext_vector_type(8))) short s16x8;
typedef __attribute__((ext_vector_type(2))) float f32x2;
typedef __attribute__((ext_vector_type(4))) float f32x4;
typedef __attribute__((ext_vector_type(16))) float f32x16;
typedef __attribute__((ext_vector_type(4))) uint32_t u32x4;
typedef __attribute__((ext_vector_type(2))) uint32_t u32x2;

#define AS3 __attribute__((address_space(3)))
#define DEVI __device__ __forceinline__

constexpr int SEQ = 2048, DM = 512, FF = 1376, NH = 32;
constexpr float LN_EPS = 1e-5f;
constexpr float LOG2E = 1.4426950408889634f;


constexpr int HTOK = SEQ + 4;
constexpr int KOG = 32 + FF / 16;
constexpr size_t GAP = 256;
constexpr size_t WS_K = GAP;
constexpr size_t WS_V = WS_K + (size_t)NH * 64 * 1024 + GAP;
constexpr size_t WS_H = WS_V + (size_t)NH * 64 * 1024 + GAP;
constexpr size_t WS_X1 = WS_H + (size_t)32 * 2 * HTOK * 16 + GAP;
constexpr size_t WS_X1S = WS_X1 + (size_t)32 * 2 * SEQ * 16 + GAP;
constexpr size_t WS_OG = WS_X1S + (size_t)16 * SEQ * 8 + GAP;
constexpr size_t WS_W2F = WS_OG + (size_t)KOG * 2 * SEQ * 16 + GAP;
constexpr size_t WS_W3F = WS_W2F + (size_t)16 * 32 * 1024 + GAP;
constexpr size_t WS_W4F = WS_W3F + (size_t)43 * 32 * 1024 + GAP;
constexpr size_t WS_EP = WS_W4F + (size_t)16 * KOG * 1024 + GAP;
constexpr size_t WS_ST = WS_EP + 256;
constexpr size_t WS_FA = WS_ST + (size_t)SEQ * 16;
constexpr size_t WS_FB = WS_FA + 32 * 128;
constexpr size_t WS_FX = WS_FB + 256 * 8;
constexpr size_t WS_FG = WS_FX + 32 * 128;
constexpr size_t WS_FO = WS_FG + 32 * 128;
constexpr size_t WS_END = WS_FO + 256 * 8;
DEVI uint32_t pk2(float a, float b) {
  f32x2 v = {a, b};
  bf16x2 r = __builtin_convertvector(v, bf16x2);
  return __builtin_bit_cast(uint32_t, r);
}
DEVI float bf_lo(uint32_t u) { return __builtin_bit_cast(float, u << 16); }
DEVI float bf_hi(uint32_t u) { return __builtin_bit_cast(float, u & 0xffff0000u); }
DEVI float dot2_ones(uint32_t u, float c) {
  bf16x2 ones = {(__bf16)1.0f, (__bf16)1.0f};
  return __builtin_amdgcn_fdot2_f32_bf16(__builtin_bit_cast(bf16x2, u), ones, c, false);
}
DEVI float dot2_self(uint32_t u, float c) {
  bf16x2 a = __builtin_bit_cast(bf16x2, u);
  return __builtin_amdgcn_fdot2_f32_bf16(a, a, c, false);
}
DEVI f32x16 mfma32(bf16x8 a, bf16x8 b, f32x16 c) {
  return __builtin_amdgcn_mfma_f32_32x32x16_bf16(a, b, c, 0, 0, 0);
}
DEVI uint32_t lds_addr(const char* p) { return (uint32_t)(uintptr_t)(const AS3 char*)p; }
DEVI void glds16(const void* sbase, uint32_t voff, uint32_t lds_dst) {
  uint32_t keep;
  asm volatile(
      "s_mov_b32 %0, m0\n\ts_mov_b32 m0, %3\n\ts_nop 0\n\tglobal_load_lds_dwordx4 %1, %2\n\ts_mov_b32 m0, %0"
      : "=&s"(keep)
      : "v"(voff), "s"(sbase), "s"(lds_dst));
}
#define WAIT_VM(n) asm volatile("s_waitcnt vmcnt(" #n ")" ::: "memory")
template <int N>
DEVI void wait_vm() {
  asm volatile("s_waitcnt vmcnt(%0)" ::"i"(N) : "memory");
}
DEVI void bar_sync() {
  asm volatile("s_waitcnt lgkmcnt(0)\n\ts_barrier" ::: "memory");
}
DEVI bf16x8 lds_b128(const char* p) { return *(const bf16x8*)p; }
DEVI bf16x8 lds_tr2(const char* p0, const char* p1) {
  s16x4 a = __builtin_amdgcn_ds_read_tr16_b64_v4i16((AS3 s16x4*)p0);
  s16x4 b = __builtin_amdgcn_ds_read_tr16_b64_v4i16((AS3 s16x4*)p1);
  s16x8 c = __builtin_shufflevector(a, b, 0, 1, 2, 3, 4, 5, 6, 7);
  return __builtin_bit_cast(bf16x8, c);
}
DEVI float gelu_tanh(float x) {
  const float c0 = -2.0f * 0.7978845608028654f * LOG2E;
  const float c1 = c0 * 0.044715f;
  float t = x * __builtin_fmaf(x * x, c1, c0);
  float e = __builtin_amdgcn_exp2f(t);
  return x * __builtin_amdgcn_rcpf(1.0f + e);
}
DEVI f32x2 gelu2(f32x2 x) {
  const float k0 = -2.0f * 0.7978845608028654f * LOG2E, k1 = k0 * 0.044715f;
  const f32x2 c0 = {k0, k0}, c1 = {k1, k1}, one = {1.0f, 1.0f};
  const f32x2 t = x * __builtin_elementwise_fma(x * x, c1, c0);
  const f32x2 d = f32x2{__builtin_amdgcn_exp2f(t[0]), __builtin_amdgcn_exp2f(t[1])} + one;
  return x * f32x2{__builtin_amdgcn_rcpf(d[0]), __builtin_amdgcn_rcpf(d[1])};
}
DEVI u32x4 pack8(const f32x16& a, int s) {
  u32x4 r;
  r[0] = pk2(a[8 * s + 0], a[8 * s + 1]);
  r[1] = pk2(a[8 * s + 2], a[8 * s + 3]);
  r[2] = pk2(a[8 * s + 4], a[8 * s + 5]);
  r[3] = pk2(a[8 * s + 6], a[8 * s + 7]);
  return r;
}
DEVI float u2f(uint32_t u) { return __builtin_bit_cast(float, u); }
DEVI uint32_t f2u(float f) { return __builtin_bit_cast(uint32_t, f); }
DEVI void swap32(float a, float b, float& ra, float& rb) {
  auto r = __builtin_amdgcn_permlane32_swap(f2u(a), f2u(b), false, false);
  uint32_t r0 = r[0], r1 = r[1];
  ra = u2f(r0);
  rb = u2f(r1);
}
DEVI float xor32(float v) {
  return __shfl_xor(v, 32, 64);
}

DEVI void glds16v(const void* vaddr, uint32_t lds_dst) {
  uint32_t keep;
  asm volatile(
      "s_mov_b32 %0, m0\n\ts_mov_b32 m0, %2\n\ts_nop 0\n\tglobal_load_lds_dwordx4 %1, off\n\ts_mov_b32 m0, %0"
      : "=&s"(keep)
      : "v"(vaddr), "s"(lds_dst));
}
DEVI f32x4 mfma16(bf16x8 a, bf16x8 b, f32x4 c) {
  return __builtin_amdgcn_mfma_f32_16x16x32_bf16(a, b, c, 0, 0, 0);
}

DEVI void st128(u32x4 v, const __amdgpu_buffer_rsrc_t r, uint32_t voff, uint32_t soff) {
  asm volatile("" : "+v"(v));
  __builtin_amdgcn_raw_buffer_store_b128(v, r, voff, soff, 16);
  asm volatile("s_nop 1" ::"v"(v));
}

constexpr int P1_XS = 32768;
constexpr int P1_W = 3 * P1_XS;
constexpr int P1_AUX = 131072;
constexpr int L_K = 0, L_V = 65536;
constexpr int L_Z = 32768;
constexpr int L_ZW = 16 * 36 * 4;
constexpr int L_Q = 131072;
constexpr int L_CP = P1_W;
constexpr int L_WV = L_CP + 1024;
constexpr int K2_SYN = L_Q + 17408;
constexpr int K2_CS = K2_SYN + 128;
constexpr int K2_EP = K2_SYN + 1024;
constexpr int L_SR = K2_SYN + 4096;
constexpr int K2_LDS = L_SR + 8192;

typedef __attribute__((address_space(1))) uint32_t gu32;
typedef __attribute__((address_space(1))) uint64_t gu64;

struct AuxUnit {
  const char* src;
  uint32_t dst;
  uint32_t stride;
  uint32_t unstride;
  int k0, kmax, n0, nmax, kkv, unv;
};
DEVI AuxUnit aux_decode(int u, const bf16_t* w2, const bf16_t* w3, const bf16_t* wo, const bf16_t* w4) {
  AuxUnit a;
  if (u < 304) {
    const int v = (u < 128) ? (u & 63) : (u - 128);
    const int kb = v >> 3, nb = v & 7;
    a.src = (const char*)(u < 64 ? w2 : (u < 128 ? wo : w4));
    a.stride = DM * 2;
    a.k0 = kb * 64;
    a.kmax = (u < 128) ? (DM - 1) : (FF - 1);
    a.n0 = nb * 64;
    a.nmax = DM - 8;
    const int kq = (u < 64) ? 32 : KOG;
    const int kofs = (u < 128) ? 0 : 32;
    a.dst = (uint32_t)(u < 64 ? WS_W2F : WS_W4F) + (uint32_t)((2 * nb) * kq + kofs + 4 * kb) * 1024;
    a.unstride = kq * 1024;
    a.kkv = (u < 128) ? 4 : min(4, FF / 16 - 4 * kb);
    a.unv = 2;
  } else {
    const int v = u - 304;
    const int kb = v & 7, nb = v >> 3;
    a.src = (const char*)w3;
    a.stride = FF * 2;
    a.k0 = kb * 64;
    a.kmax = DM - 1;
    a.n0 = nb * 64;
    a.nmax = FF - 8;
    a.dst = (uint32_t)WS_W3F + (uint32_t)((2 * nb) * 32 + 4 * kb) * 1024;
    a.unstride = 32 * 1024;
    a.kkv = 4;
    a.unv = min(2, 43 - 2 * nb);
  }
  return a;
}

DEVI void glds4c(const void* sbase, uint32_t voff, uint32_t lds_dst) {
  uint32_t keep;
  asm volatile(
      "s_mov_b32 %0, m0\n\ts_mov_b32 m0, %3\n\ts_nop 0\n\tglobal_load_lds_dword %1, %2 sc1\n\ts_mov_b32 m0, %0"
      : "=&s"(keep)
      : "v"(voff), "s"(sbase), "s"(lds_dst));
}
DEVI void glds4(const void* sbase, uint32_t voff, uint32_t lds_dst) {
  uint32_t keep;
  asm volatile(
      "s_mov_b32 %0, m0\n\ts_mov_b32 m0, %3\n\ts_nop 0\n\tglobal_load_lds_dword %1, %2\n\ts_mov_b32 m0, %0"
      : "=&s"(keep)
      : "v"(voff), "s"(sbase), "s"(lds_dst));
}
template <int CTRL, int RM>
DEVI float dpp_add(float v) {
  return v + u2f(__builtin_amdgcn_update_dpp(0u, f2u(v), CTRL, RM, 0xf, true));
}
DEVI float wave_sum(float v) {
  v = dpp_add<0x111, 0xf>(v);
  v = dpp_add<0x112, 0xf>(v);
  v = dpp_add<0x114, 0xf>(v);
  v = dpp_add<0x118, 0xf>(v);
  v = dpp_add<0x142, 0xa>(v);
  v = dpp_add<0x143, 0xc>(v);
  return u2f(__builtin_amdgcn_readlane(f2u(v), 63));
}

DEVI void wave_sum2(float& a, float& b) {
  a = dpp_add<0x111, 0xf>(a); b = dpp_add<0x111, 0xf>(b);
  a = dpp_add<0x112, 0xf>(a); b = dpp_add<0x112, 0xf>(b);
  a = dpp_add<0x114, 0xf>(a); b = dpp_add<0x114, 0xf>(b);
  a = dpp_add<0x118, 0xf>(a); b = dpp_add<0x118, 0xf>(b);
  a = dpp_add<0x142, 0xa>(a); b = dpp_add<0x142, 0xa>(b);
  a = dpp_add<0x143, 0xc>(a); b = dpp_add<0x143, 0xc>(b);
  a = u2f(__builtin_amdgcn_readlane(f2u(a), 63));
  b = u2f(__builtin_amdgcn_readlane(f2u(b), 63));
}


DEVI uint64_t phase1(char* smem, const bf16_t* __restrict__ x, const bf16_t* __restrict__ wqkv, const bf16_t* __restrict__ w1,
                     const bf16_t* __restrict__ w2, const bf16_t* __restrict__ w3, const bf16_t* __restrict__ wo,
                     const bf16_t* __restrict__ w4, char* __restrict__ ws, const bf16_t* __restrict__ out,
                     const __amdgpu_buffer_rsrc_t rws, int b, int head, int qg, int wid, int lane) {
  const uint32_t lds0 = lds_addr(smem);
  const int r8 = lane >> 3, c8 = lane & 7;
  const int gc = c8 ^ (((r8 >> 1) & 1) << 2);
  const int hh = lane >> 5, l31 = lane & 31;
  const int g2 = lane >> 4, q = (lane >> 2) & 3, p4 = lane & 3;
  const int rot = ((2 * qg + (head & 1)) & 7);
  const AuxUnit a0 = aux_decode(b, w2, w3, wo, w4);
  const bool has1 = b + 256 < 480;
  const AuxUnit a1 = aux_decode(has1 ? b + 256 : b, w2, w3, wo, w4);
  auto launch_tag = [&]() {
    const uint64_t ep = *(const uint64_t*)(smem + K2_EP);
    const uint64_t eu = ((uint64_t)(uint32_t)__builtin_amdgcn_readfirstlane((uint32_t)(ep >> 32)) << 32) |
                        (uint32_t)__builtin_amdgcn_readfirstlane((uint32_t)ep);
    uint64_t t = eu * 6364136223846793005ull + 1442695040888963407ull;
    if ((uint32_t)t == 0u) t += 1;
    return t;
  };
  uint64_t tag;
  if (wid >= 4) {
    const int p = wid - 4;
    const int kr = p * 16 + r8;
    const char* xsrc = (const char*)(x + (size_t)(qg * 256 + p * 64) * DM);
    const uint32_t xo = r8 * 1024 + ((c8 ^ ((r8 >> 1) & 3)) << 4);
    const int seg = gc >> 1;
    const char* wp = seg < 3 ? (const char*)(wqkv + seg * 512 + head * 16 + (gc & 1) * 8) + (size_t)kr * 3072
                             : (const char*)(w1 + head * 16 + (gc & 1) * 8) + (size_t)kr * 1024;
    const uint32_t w8 = seg < 3 ? 8 * 3072 : 8 * 1024;
    auto dma_stage = [&](int s) {
      const char* wq = wp + (size_t)(((s + rot) & 7) * 8 * w8);
      glds16v(wq, lds0 + P1_W + (s & 3) * 8192 + p * 2048);
      glds16v(wq + w8, lds0 + P1_W + (s & 3) * 8192 + p * 2048 + 1024);
#pragma unroll
      for (int i = 0; i < 8; ++i) {
        glds16(xsrc + ((s + rot) & 7) * 128, (xo ^ ((i & 1) << 6)) + i * 8192, lds0 + (s % 3) * P1_XS + p * 8192 + i * 1024);
      }
    };
    const int stok = qg * 256 + head * 8 + 2 * p;
    dma_stage(0);
    glds16(x + (size_t)stok * DM, lane * 16, lds0 + L_SR + p * 2048);
    glds16(x + (size_t)stok * DM, 1024 + lane * 16, lds0 + L_SR + p * 2048 + 1024);
    glds4c(ws + WS_EP, lane * 4, lds0 + K2_EP);
    dma_stage(1);
    wait_vm<13>();
    bar_sync();
    dma_stage(2);
    wait_vm<20>();
    tag = launch_tag();
#pragma unroll
    for (int t = 0; t < 2; ++t) {
      const u32x4 xr = *(const u32x4*)(smem + L_SR + p * 2048 + t * 1024 + lane * 16);
      float sx = 0.f, sq = 0.f;
#pragma unroll
      for (int j = 0; j < 4; ++j) { sx = dot2_ones(xr[j], sx); sq = dot2_self(xr[j], sq); }
      wave_sum2(sx, sq);
      const float mean = sx * (1.0f / DM);
      const float var = fmaxf(sq * (1.0f / DM) - mean * mean, 0.f);
      const float rstd = __builtin_amdgcn_rsqf(var + LN_EPS);
      const float rh = rstd * __builtin_amdgcn_rsqf(var * rstd * rstd + LN_EPS);
      if (lane == 0) st128(u32x4{f2u(mean), f2u(rstd), f2u(rh), (uint32_t)tag}, rws, (stok + t) * 16, (uint32_t)WS_ST);
    }
    wait_vm<12>();
    bar_sync();
#pragma unroll
    for (int s = 0; s < 7; ++s) {
      if (s + 3 < 8) dma_stage(s + 3);
      if (s == 0) wait_vm<12>();
      else if (s + 3 < 8) wait_vm<10>();
      else if (s == 5) wait_vm<0>();
      if (s == 6) {
        const int ao = P1_AUX + p * 2048 + (4 * hh + q) * 128 + (((g2 & 1) * 32 + 8 * p4) ^ (64 * (q >> 1)));
        u32x4 af[4];
#pragma unroll
        for (int i = 0; i < 4; ++i) {
          const char* pa = smem + (ao ^ ((i & 1) << 6)) + (i >> 1) * 8192;
          af[i] = __builtin_bit_cast(u32x4, lds_tr2(pa, pa + 1024));
        }
        if (p < a0.kkv) {
          st128(af[0], rws, lane * 16, a0.dst + p * 1024);
          if (a0.unv > 1) st128(af[1], rws, lane * 16, a0.dst + a0.unstride + p * 1024);
        }
        if (has1 && p < a1.kkv) {
          st128(af[2], rws, lane * 16, a1.dst + p * 1024);
          if (a1.unv > 1) st128(af[3], rws, lane * 16, a1.dst + a1.unstride + p * 1024);
        }
      }
      bar_sync();
    }
  } else {
    auto warm = [&]() {
    if (wid == 0) {
      uint64_t pc;
      uint32_t len;
      asm volatile("s_getpc_b64 %0\n.Lpf_base:\n\ts_mov_b32 %1, .Lpf_end-.Lpf_base" : "=s"(pc), "=s"(len));
      glds4((const void*)pc, min((uint32_t)(((b >> 3) & 15) * 4096 + lane * 64), len - 4u), lds0 + K2_SYN + 3072);
    }
    if (wid == 1) {
      uint32_t o = 0;
      o = (lane & 7) == 1 ? (uint32_t)(WS_V - GAP) : o;
      o = (lane & 7) == 2 ? (uint32_t)(WS_H - GAP) : o;
      o = (lane & 7) == 3 ? (uint32_t)(WS_X1 - GAP) : o;
      o = (lane & 7) == 4 ? (uint32_t)(WS_OG - GAP) : o;
      o = (lane & 7) == 5 ? (uint32_t)(WS_W2F - GAP) : o;
      o = (lane & 7) == 6 ? (uint32_t)(WS_W4F - GAP) : o;
      glds4(ws, o, lds0 + K2_SYN + 3072 + 256);
    }
    if (wid == 2) glds4(out, b * 8192, lds0 + K2_SYN + 3072 + 512);
    };
    warm();
    auto dma_aux = [&]() {
#pragma unroll
      for (int i = 0; i < 2; ++i) {
        const int kr = wid * 16 + i * 8 + r8;
        glds16(a0.src, (uint32_t)min(a0.k0 + kr, a0.kmax) * a0.stride + (uint32_t)min(a0.n0 + gc * 8, a0.nmax) * 2,
               lds0 + P1_AUX + wid * 2048 + i * 1024);
        glds16(a1.src, (uint32_t)min(a1.k0 + kr, a1.kmax) * a1.stride + (uint32_t)min(a1.n0 + gc * 8, a1.nmax) * 2,
               lds0 + P1_AUX + 8192 + wid * 2048 + i * 1024);
      }
    };
    const int xsw = hh ^ ((l31 >> 1) & 7);
    const int xrow = wid * 8192 + l31 * 128;
    const int wb0 = P1_W + (8 * hh + q) * 128 + (((g2 & 1) * 32 + 8 * p4) ^ (64 * (q >> 1)));
    const uint32_t pv = ((lane & 15) == ((lane >> 4) & 1)) ? 0x3f803f80u : 0u;
    const bf16x8 pat = __builtin_bit_cast(bf16x8, u32x4{pv, pv, pv, pv});
    bf16x8 xf[2][4][2], w0[2][4], w1f[2][4];
    auto rd_x = [&](int set, int s, int kk) {
#pragma unroll
      for (int t = 0; t < 2; ++t) xf[set][kk][t] = lds_b128(smem + (s % 3) * P1_XS + xrow + t * 4096 + ((xsw ^ (2 * kk)) << 4));
    };
    auto rd_w0 = [&](int set, int s, int kk) {
      const char* pw = smem + (s & 3) * 8192 + wb0 + kk * 2048;
      w0[set][kk] = lds_tr2(pw, pw + 512);
    };
    auto rd_w1 = [&](int set, int s, int kk) {
      const char* pw = smem + (s & 3) * 8192 + (wb0 ^ 64) + kk * 2048;
      w1f[set][kk] = lds_tr2(pw, pw + 512);
    };
    f32x16 acc0[2] = {}, acc1[2] = {};
    f32x4 cs0 = {}, cs1 = {};
    auto colsum = [&](int cur) {
      switch (wid) {
        case 0: cs0 = mfma16(pat, w0[cur][0], cs0); cs1 = mfma16(pat, w1f[cur][0], cs1); break;
        case 1: cs0 = mfma16(pat, w0[cur][1], cs0); cs1 = mfma16(pat, w1f[cur][1], cs1); break;
        case 2: cs0 = mfma16(pat, w0[cur][2], cs0); cs1 = mfma16(pat, w1f[cur][2], cs1); break;
        default: cs0 = mfma16(pat, w0[cur][3], cs0); cs1 = mfma16(pat, w1f[cur][3], cs1); break;
      }
    };
    u32x4 se[2];
    f32x4 cq[4];
    float cv = 0.f;
    bar_sync();
#pragma unroll
    for (int kk = 0; kk < 4; ++kk) { rd_x(0, 0, kk); rd_w0(0, 0, kk); rd_w1(0, 0, kk); }
#pragma unroll
    for (int s = 0; s < 8; ++s) {
      const int cur = s & 1, nxt = cur ^ 1;
      if (s == 6) {
        wait_vm<0>();
        asm volatile("" : "+v"(se[0]), "+v"(se[1]));
      }
      bar_sync();
      if (s == 0) tag = launch_tag();
      if (s == 1) dma_aux();
      if (s == 5) {
#pragma unroll
        for (int t = 0; t < 2; ++t)
          se[t] = __builtin_amdgcn_raw_buffer_load_b128(rws, (qg * 256 + wid * 64 + t * 32 + l31) * 16, (uint32_t)WS_ST, 16);
      }
      if (s == 6) {
        for (uint32_t spin = 0; spin < (1u << 16); ++spin) {
          if (__all(se[0][3] == (uint32_t)tag && se[1][3] == (uint32_t)tag)) break;
          __builtin_amdgcn_s_sleep(1);
          asm volatile("" ::: "memory");
#pragma unroll
          for (int t = 0; t < 2; ++t)
            se[t] = __builtin_amdgcn_raw_buffer_load_b128(rws, (qg * 256 + wid * 64 + t * 32 + l31) * 16, (uint32_t)WS_ST, 16);
        }
        if (hh == 0) {
#pragma unroll
          for (int t = 0; t < 2; ++t) {
            float* wv = (float*)(smem + L_WV + (wid * 2 + t) * 1024);
            wv[l31] = u2f(se[t][0]);
            wv[32 + l31] = u2f(se[t][1]);
            wv[64 + l31] = u2f(se[t][2]);
          }
        }
      }
      if (s == 7) {
        const f32x4 a = *(const f32x4*)(smem + L_CP + lane * 16);
        float* wv = (float*)(smem + L_WV + wid * 2048);
        wv[96 + lane] = (a[0] + a[1]) + (a[2] + a[3]);
      }
      __builtin_amdgcn_sched_barrier(0);
#pragma unroll
      for (int kk = 0; kk < 4; ++kk) {
        acc0[0] = mfma32(w0[cur][kk], xf[cur][kk][0], acc0[0]);
        if (s < 7) rd_x(nxt, s + 1, kk);
        __builtin_amdgcn_sched_barrier(0);
        acc1[0] = mfma32(xf[cur][kk][0], w1f[cur][kk], acc1[0]);
        if (s < 7) rd_w0(nxt, s + 1, kk);
        __builtin_amdgcn_sched_barrier(0);
        acc0[1] = mfma32(w0[cur][kk], xf[cur][kk][1], acc0[1]);
        if (s < 7) rd_w1(nxt, s + 1, kk);
        __builtin_amdgcn_sched_barrier(0);
        acc1[1] = mfma32(xf[cur][kk][1], w1f[cur][kk], acc1[1]);
        if (s == 7 && kk == 1) {
          const float* wv = (const float*)(smem + L_WV + wid * 2048);
#pragma unroll
          for (int g = 0; g < 4; ++g) cq[g] = *(const f32x4*)(wv + 96 + 8 * g + 4 * hh);
          cv = wv[96 + 32 + l31];
        }
        __builtin_amdgcn_sched_barrier(0);
      }
      if (s == 0) colsum(cur);
      if (s < 7) colsum(nxt);
      if (s == 6 && lane < 16) {
        float* cp = (float*)(smem + L_CP) + lane * 4 + wid;
        cp[0] = cs0[0];
        cp[64] = cs0[1];
        cp[128] = cs1[0];
        cp[192] = cs1[1];
      }
    }
    asm volatile("" : "+v"(acc0[0]), "+v"(acc1[0]), "+v"(acc0[1]), "+v"(acc1[1]));
#pragma unroll
    for (int t = 0; t < 2; ++t) {
      const int ti = wid * 2 + t;
      const float mean = u2f(se[t][0]), rstd = u2f(se[t][1]);
      {
        f32x16 v;
#pragma unroll
        for (int g = 0; g < 4; ++g) {
          const float rs = g < 2 ? rstd * (0.25f * LOG2E) : rstd;
#pragma unroll
          for (int j = 0; j < 4; ++j) v[4 * g + j] = (acc0[t][4 * g + j] - mean * cq[g][j]) * rs;
        }
        *(u32x4*)(smem + L_Q + ti * 1024 + lane * 16) = pack8(v, 0);
        *(u32x4*)(smem + L_K + ti * 1024 + lane * 16) = pack8(v, 1);
      }
      {
        const float* wv = (const float*)(smem + L_WV + ti * 1024);
        const float* sc = wv + (l31 < 16 ? 32 : 64);
        f32x16 v;
#pragma unroll
        for (int g = 0; g < 4; ++g) {
          const f32x4 m4 = *(const f32x4*)(wv + 8 * g + 4 * hh);
          const f32x4 r4 = *(const f32x4*)(sc + 8 * g + 4 * hh);
#pragma unroll
          for (int j = 0; j < 4; ++j) v[4 * g + j] = (acc1[t][4 * g + j] - m4[j] * cv) * r4[j];
        }
        if (l31 < 16) {
          char* dst = smem + L_V + ti * 1024 + hh * 256 + l31 * 16;
#pragma unroll
          for (int s = 0; s < 2; ++s) *(u32x4*)(dst + s * 512) = pack8(v, s);
        } else {
          char* dst = smem + L_Z + ti * L_ZW + (l31 - 16) * 144 + hh * 16;
#pragma unroll
          for (int g = 0; g < 4; ++g) *(f32x4*)(dst + g * 32) = f32x4{v[4 * g], v[4 * g + 1], v[4 * g + 2], v[4 * g + 3]};
        }
      }
    }
  }
  bar_sync();
  return tag;
}
#define RK(i) "v[104+12*" #i ":104+12*" #i "+3]"
#define RV0(i) "v[108+12*" #i ":108+12*" #i "+3]"
#define RV1(i) "v[112+12*" #i ":112+12*" #i "+3]"
#define AT_E(d, s) "v_exp_f32 v[" d "], v[" s "]\n"
#define AT_M(d, s) "v_mov_b32_sdwa v[" d "], v[" s "] dst_sel:WORD_0 dst_unused:UNUSED_PRESERVE src0_sel:WORD_1\n"
#define AT_UNIT(SC, SN, KN, QN, NN, V1P, OP, V0C, OC, X0, X1, X2)                                     \
  X0 "v_mfma_f32_32x32x16_bf16 v[" SN ":" SN "+15], " KN ", " QN ", " NN "\n"                          \
  AT_E("96", SC "+1") X1 AT_E(SC "+0", SC "+0") X2 AT_E("97", SC "+3") AT_E(SC "+2", SC "+2")          \
  AT_M("96", SC "+0") AT_E("98", SC "+5") AT_E(SC "+4", SC "+4") AT_M("97", SC "+2")                   \
  "v_mfma_f32_32x32x16_bf16 " OP ", " V1P ", v[100:103], " OP "\n"                                     \
  AT_E("99", SC "+7") AT_E(SC "+6", SC "+6") AT_M("98", SC "+4") AT_E("100", SC "+9")                  \
  AT_M("99", SC "+6") AT_E(SC "+8", SC "+8") AT_E("101", SC "+11")                                     \
  "v_mfma_f32_32x32x16_bf16 " OC ", " V0C ", v[96:99], " OC "\n"                                       \
  AT_E(SC "+10", SC "+10") AT_M("100", SC "+8") AT_E("102", SC "+13") AT_E(SC "+12", SC "+12")         \
  AT_M("101", SC "+10") AT_E("103", SC "+15") AT_E(SC "+14", SC "+14") AT_M("102", SC "+12")           \
  AT_M("103", SC "+14")
#define AT_LK(slot, off) "ds_read_b128 " RK(slot) ", %[vk] offset:" off "\n"
#define AT_LV(slot, off)                                                                               \
  "s_mov_b64 exec, %[msk]\n ds_read_b128 " RV0(slot) ", %[vv] offset:" off "\n ds_read_b128 " RV1(slot) \
  ", %[vv] offset:" off "+512\n s_mov_b64 exec, -1\n"
#define AT_STEP(i, n, p, j)                                                                            \
  AT_UNIT("64", "80", RK(i), "v[188:191]", "v[168:183]", RV1(p), "%[ob]", RV0(i), "%[oa]", "", "", "") \
  AT_UNIT("80", "64", RK(n), "v[184:187]", "v[152:167]", RV1(i), "%[oa]", RV0(i), "%[ob]",             \
          "s_waitcnt lgkmcnt(3)\n", AT_LK(p, "1024*" #j "+3072"), AT_LV(p, "1024*" #j "+3072"))
#define AT_INITP AT_MOV4("100", "0")
#define AT_FINAL "v_mfma_f32_32x32x16_bf16 %[ob], " RV1(3) ", v[100:103], %[ob]\n"
#define AT_L3(slot, j) AT_LK(slot, "1024*" #j) AT_LV(slot, "1024*" #j)
#define AT_MAX16(d, s)                                                                                 \
  "v_max3_f32 v" d ", v[" s "+0], v[" s "+1], v[" s "+2]\n"                                            \
  "v_max3_f32 v" d ", v" d ", v[" s "+3], v[" s "+4]\n"                                                \
  "v_max3_f32 v" d ", v" d ", v[" s "+5], v[" s "+6]\n"                                                \
  "v_max3_f32 v" d ", v" d ", v[" s "+7], v[" s "+8]\n"                                                \
  "v_max3_f32 v" d ", v" d ", v[" s "+9], v[" s "+10]\n"                                               \
  "v_max3_f32 v" d ", v" d ", v[" s "+11], v[" s "+12]\n"                                              \
  "v_max3_f32 v" d ", v" d ", v[" s "+13], v[" s "+14]\n"                                              \
  "v_max_f32 v" d ", v" d ", v[" s "+15]\n"
#define AT_MOV4(d, s) "v_mov_b32 v[" d "+0], " s "\n v_mov_b32 v[" d "+1], " s "\n v_mov_b32 v[" d "+2], " s "\n v_mov_b32 v[" d "+3], " s "\n"
#define AT_MOV16(d, s) AT_MOV4(d, s) AT_MOV4(d "+4", s) AT_MOV4(d "+8", s) AT_MOV4(d "+12", s)
#define AT_SYNC(lbl, off)                                                                              \
  lbl ":\n ds_read_b32 v192, %[sy] offset:" off "\n s_waitcnt lgkmcnt(0)\n"                            \
  "v_readfirstlane_b32 %[tmp], v192\n s_cmp_lt_u32 %[tmp], 4\n s_cbranch_scc1 " lbl "b\n"

DEVI uint32_t lds_add_rtn(uint32_t addr) {
  uint32_t r, one = 1;
  uint64_t keep;
  asm volatile(
      "s_waitcnt lgkmcnt(0)\n\ts_mov_b64 %1, exec\n\ts_mov_b64 exec, 1\n\tds_add_rtn_u32 %0, %2, %3\n\t"
      "s_waitcnt lgkmcnt(0)\n\ts_mov_b64 exec, %1"
      : "=&v"(r), "=&s"(keep)
      : "v"(addr), "v"(one)
      : "memory");
  return __builtin_amdgcn_readfirstlane(r);
}
DEVI void lds_wait_ge(uint32_t addr, uint32_t target) {
  for (;;) {
    uint32_t v;
    asm volatile("ds_read_b32 %0, %1\n\ts_waitcnt lgkmcnt(0)" : "=v"(v) : "v"(addr) : "memory");
    if (__builtin_amdgcn_readfirstlane(v) >= target) break;
    __builtin_amdgcn_s_sleep(1);
  }
}
DEVI void role_bar(uint32_t addr) {
  lds_add_rtn(addr);
  lds_wait_ge(addr, 4);
}

DEVI void attention_wave(char* smem, char* __restrict__ ws, const __amdgpu_buffer_rsrc_t rws, uint64_t tag, int head, int qtile0,
                         int wid, int lane, uint32_t sa) {
  f32x16 oa = {}, ob = {};
  const uint32_t lds0 = lds_addr(smem);
  {
    const uint32_t vq = lds0 + L_Q + wid * 2048 + lane * 16;
    uint32_t vk = lds0 + L_K + lane * 16;
    uint32_t vv = lds0 + L_V + (lane >> 5) * 256 + (lane & 15) * 16;
    const uint32_t sy = lds0 + K2_SYN + 16;
    uint32_t cnt, tmp;
    const uint32_t o2 = ((lane & 27) == 16) ? 0x3f803f80u : 0u;
    asm volatile(
        "ds_read_b128 v[184:187], %[vq]\n"
        "ds_read_b128 v[188:191], %[vq] offset:1024\n"
        "ds_read_b128 " RK(3) ", %[vk]\n"
        AT_INITP AT_MOV4("148", "%[o2]")
        AT_MOV4("108", "%[o2]") AT_MOV4("112", "%[o2]") AT_MOV4("120", "%[o2]") AT_MOV4("124", "%[o2]")
        AT_MOV4("132", "%[o2]") AT_MOV4("136", "%[o2]") AT_MOV4("144", "%[o2]")
        "s_mov_b32 %[cnt], 0\n"
        "s_waitcnt lgkmcnt(0)\n"
        "v_mfma_f32_32x32x16_bf16 v[64:79], " RK(3) ", v[184:187], 0\n"
        "v_mfma_f32_32x32x16_bf16 v[80:95], " RK(3) ", v[188:191], 0\n"
        AT_L3(0, 0) AT_L3(1, 1) AT_L3(2, 2)
        "s_nop 7\n s_nop 7\n"
        AT_MAX16("192", "64")
        "s_nop 3\n"
        AT_MAX16("193", "80")
        "v_mov_b32 v194, v192\n v_mov_b32 v195, v193\n s_nop 1\n"
        "v_permlane32_swap_b32 v192, v194\n"
        "v_permlane32_swap_b32 v193, v195\n"
        "s_nop 1\n"
        "v_max_f32 v192, v192, v194\n v_max_f32 v193, v193, v195\n"
        "v_mul_f32 v192, -1.0, v192\n v_mul_f32 v193, -1.0, v193\n"
        AT_MOV16("152", "v192") AT_MOV16("168", "v193")
        "s_nop 1\n s_branch 3f\n"
        "2:\n"
        AT_L3(0, 0) AT_L3(1, 1) AT_L3(2, 2)
        "3:\n"
        "s_waitcnt lgkmcnt(6)\n"
        "v_mfma_f32_32x32x16_bf16 v[64:79], " RK(0) ", v[184:187], v[152:167]\n"
        "s_nop 7\n s_nop 7\n s_nop 3\n"
        "1:\n"
        AT_STEP(0, 1, 3, 0) AT_STEP(1, 2, 0, 1) AT_STEP(2, 3, 1, 2) AT_STEP(3, 0, 2, 3)
        AT_STEP(0, 1, 3, 4) AT_STEP(1, 2, 0, 5) AT_STEP(2, 3, 1, 6) AT_STEP(3, 0, 2, 7)
        "v_add_u32 %[vk], 8192, %[vk]\n"
        "v_add_u32 %[vv], 8192, %[vv]\n"
        "s_add_u32 %[cnt], %[cnt], 1\n"
        "s_cmp_lg_u32 %[cnt], 1\n"
        "s_cbranch_scc1 5f\n"
        "s_waitcnt lgkmcnt(0)\n"
        AT_SYNC("4", "0")
        "s_branch 2b\n"
        "5:\n"
        "s_cmp_lg_u32 %[cnt], 2\n"
        "s_cbranch_scc1 7f\n"
        AT_SYNC("6", "4")
        "7:\n"
        "s_cmp_lg_u32 %[cnt], 4\n"
        "s_cbranch_scc1 9f\n"
        "v_mov_b32 v193, 1\n s_mov_b64 exec, 1\n ds_add_u32 %[sy], v193 offset:48\n s_mov_b64 exec, -1\n"
        AT_SYNC("8", "8")
        "9:\n"
        "s_cmp_lt_u32 %[cnt], 8\n"
        "s_cbranch_scc1 1b\n"
        AT_FINAL
        "s_waitcnt lgkmcnt(0)\n"
        "s_nop 7\n s_nop 7\n"
        : [oa] "+&v"(oa), [ob] "+&v"(ob), [vk] "+&v"(vk), [vv] "+&v"(vv), [cnt] "=&s"(cnt), [tmp] "=&s"(tmp)
        : [vq] "v"(vq), [msk] "s"(0x0000ffff0000ffffull), [sy] "v"(sy), [o2] "v"(o2)
        : "memory", "scc",
#define C10(a) "v" #a "0", "v" #a "1", "v" #a "2", "v" #a "3", "v" #a "4", "v" #a "5", "v" #a "6", "v" #a "7", "v" #a "8", "v" #a "9"
          "v64", "v65", "v66", "v67", "v68", "v69", C10(7), C10(8), C10(9), C10(10), C10(11), C10(12), C10(13), C10(14),
          C10(15), C10(16), C10(17), C10(18), "v190", "v191", "v192", "v193", "v194", "v195");
  }
#pragma unroll 1
  for (int u = 0; u < 2; ++u) {
    f32x16 o = u ? ob : oa;
    const int qtile = qtile0 + u;
    float l = o[8];
    bool bad = !(l < 1.0e30f);
#pragma unroll
    for (int r = 0; r < 8; ++r) bad = bad || !(fabsf(o[r]) < 3.0e38f);
    if (__any(bad)) {
      const uint32_t kbase = (uint32_t)WS_K + (uint32_t)head * 65536u, vbase = (uint32_t)WS_V + (uint32_t)head * 65536u;
      const bf16x8 qf = *(const bf16x8*)(smem + L_Q + (wid * 2 + u) * 1024 + lane * 16);
      f32x16 z = {};
      float m = -3.0e38f;
      o = z;
      const uint32_t ONE2 = 0x3f803f80u;
      const uint32_t vo = (lane >> 5) * 256 + (lane & 15) * 16;
#pragma unroll 1
      for (int t = 0; t < 64; ++t) {
        const bf16x8 kf = __builtin_bit_cast(bf16x8, __builtin_amdgcn_raw_buffer_load_b128(rws, lane * 16, kbase + t * 1024, 16));
        u32x4 v0 = __builtin_amdgcn_raw_buffer_load_b128(rws, vo, vbase + t * 1024, 16);
        u32x4 v1 = __builtin_amdgcn_raw_buffer_load_b128(rws, vo, vbase + t * 1024 + 512, 16);
        if (lane & 16) { v0 = u32x4{ONE2, ONE2, ONE2, ONE2}; v1 = v0; }
        f32x16 s = mfma32(kf, qf, z);
        float tm = s[0];
#pragma unroll
        for (int r = 1; r < 16; ++r) tm = fmaxf(tm, s[r]);
        tm = fmaxf(tm, xor32(tm));
        float mn = fmaxf(m, tm);
        float al = __builtin_amdgcn_exp2f(m - mn);
        m = mn;
        f32x16 pe;
#pragma unroll
        for (int r = 0; r < 16; ++r) {
          pe[r] = __builtin_amdgcn_exp2f(s[r] - mn);
          o[r] *= al;
        }
        bf16x8 p0 = __builtin_bit_cast(bf16x8, pack8(pe, 0));
        bf16x8 p1 = __builtin_bit_cast(bf16x8, pack8(pe, 1));
        o = mfma32(__builtin_bit_cast(bf16x8, v0), p0, o);
        o = mfma32(__builtin_bit_cast(bf16x8, v1), p1, o);
      }
      l = o[8];
    }
    const float inv = 1.0f / l;
    f32x16 on;
#pragma unroll
    for (int r = 0; r < 8; ++r) on[r] = o[r] * inv;
    const int tok = qtile * 32 + (lane & 31);
    st128(pack8(on, 0), rws, ((head * 2 + (lane >> 5)) * SEQ + tok) * 16, (uint32_t)WS_OG);
  }
  asm volatile("s_waitcnt vmcnt(0)" ::: "memory");
  if (lds_add_rtn(lds0 + K2_SYN + 28) == 3 && lane == 0)
    __hip_atomic_store((gu64*)(ws + WS_FO + ((qtile0 >> 3) * 32 + head) * 8), tag, __ATOMIC_RELAXED, __HIP_MEMORY_SCOPE_AGENT);
}

#define st128l st128
#define LAUX 16
#define FLAGL(p, t) __hip_atomic_store((gu64*)(p), t, __ATOMIC_RELAXED, __HIP_MEMORY_SCOPE_AGENT)
DEVI int launder(int v) {
  asm volatile("" : "+v"(v));
  return v;
}
DEVI void poll_flags(const char* f, int n, uint64_t tag, uint32_t lds_word, int wid, int lane) {
  if (wid == 0) {
    const gu64* p = (const gu64*)f + (lane & (n - 1));
    for (uint32_t spin = 0; spin < (1u << 16); ++spin) {
      if (__all(__hip_atomic_load(p, __ATOMIC_RELAXED, __HIP_MEMORY_SCOPE_AGENT) == tag)) break;
      __builtin_amdgcn_s_sleep(1);
    }
    lds_add_rtn(lds_word);
  } else {
    lds_wait_ge(lds_word, 1);
  }
  __builtin_amdgcn_fence(__ATOMIC_ACQUIRE, "wavefront");
}
DEVI void glds16c(const void* sbase, uint32_t voff, uint32_t lds_dst) {
  uint32_t keep;
  asm volatile(
      "s_mov_b32 %0, m0\n\ts_mov_b32 m0, %3\n\ts_nop 0\n\tglobal_load_lds_dwordx4 %1, %2 sc1\n\ts_mov_b32 m0, %0"
      : "=&s"(keep)
      : "v"(voff), "s"(sbase), "s"(lds_dst));
}
DEVI int blk_head(int b) { return (b & 3) * 8 + (b >> 5); }
DEVI int blk_qg(int b) { return (b & 4) + ((b >> 3) & 3); }
DEVI void mixer_wave(char* smem, const bf16_t* __restrict__ x, char* __restrict__ ws, bf16_t* __restrict__ out,
                     const __amdgpu_buffer_rsrc_t rws, uint64_t tag, int b, int head, int qg, int wid, int lane) {
  const uint32_t syn = lds_addr(smem) + K2_SYN;
  const int tg = (b & 7) * 4 + (b >> 6), nb = (b >> 3) & 7;
  int hh = lane >> 5, l31 = lane & 31;
  {
    const uint32_t kd = (uint32_t)WS_K + (uint32_t)(head * 64 + qg * 8 + wid) * 1024;
#pragma unroll
    for (int i = 0; i < 2; ++i) {
      const u32x4 kk = *(const u32x4*)(smem + L_K + (wid + 4 * i) * 1024 + lane * 16);
      const u32x4 vv = *(const u32x4*)(smem + L_V + (wid + 4 * i) * 1024 + lane * 16);
      st128(kk, rws, lane * 16, kd + i * 4096);
      st128(vv, rws, lane * 16, kd + i * 4096 + (uint32_t)(WS_V - WS_K));
    }
    asm volatile("" ::: "memory");
#pragma unroll
    for (int i = 0; i < 2; ++i) {
      const int w = wid + 4 * i;
      const float* z = (const float*)(smem + L_Z + w * L_ZW) + (4 * hh) * 36 + l31;
      u32x4 pk;
#pragma unroll
      for (int j = 0; j < 4; ++j) {
        const int c = (j >> 1) * 8 + (j & 1) * 2;
        const f32x2 g = gelu2(f32x2{z[c * 36], z[(c + 1) * 36]});
        pk[j] = pk2(g[0], g[1]);
      }
      st128(pk, rws, ((head * 2 + hh) * HTOK + qg * 256 + w * 32 + l31 + 2) * 16, (uint32_t)WS_H);
    }
    wait_vm<2>();
    if (lds_add_rtn(syn + 52) == 3 && lane == 0)
      __hip_atomic_store((gu64*)(ws + WS_FA + head * 128 + qg * 8), tag, __ATOMIC_RELAXED, __HIP_MEMORY_SCOPE_AGENT);
    if (wid == 0 && (qg == 0 || qg == 7) && lane < 4)
      st128(u32x4{0u, 0u, 0u, 0u}, rws,
                                             ((head * 2 + (lane >> 1)) * HTOK + (qg ? SEQ + 2 : 0) + (lane & 1)) * 16, (uint32_t)WS_H);
    wait_vm<0>();
    if (lds_add_rtn(syn + 56) == 3 && lane == 0)
      __hip_atomic_store((gu64*)(ws + WS_FB + (qg * 32 + head) * 8), tag, __ATOMIC_RELAXED, __HIP_MEMORY_SCOPE_AGENT);
  }
  const uint32_t kvg = (uint32_t)WS_K + (uint32_t)head * 65536u + wid * 1024 + lane * 16;
  const uint32_t kvd = lds_addr(smem) + wid * 1024;
  auto kv_copy = [&](int c) {
    const uint32_t so = (uint32_t)((((qg + (c >> 1)) & 7) << 13) + (c & 1) * 4096);
    glds16c(ws, kvg + so, kvd + L_K + c * 4096);
    glds16c(ws + (WS_V - WS_K), kvg + so, kvd + L_V + c * 4096);
  };
  auto poll_kv = [&](int n, uint32_t sig) {
    const gu64* f = (const gu64*)(ws + WS_FA + head * 128 + ((qg + lane) & 7) * 8);
    const uint64_t need = ((2ull << n) - 2ull);
    for (uint32_t spin = 0; spin < (1u << 16); ++spin) {
      const uint64_t v = __hip_atomic_load(f, __ATOMIC_RELAXED, __HIP_MEMORY_SCOPE_AGENT);
      const uint64_t ok = __ballot(v == tag);
      if (spin == 0 && sig) { wait_vm<0>(); lds_add_rtn(sig); }
      if ((ok & need) == need) break;
      __builtin_amdgcn_s_sleep(1);
    }
    __builtin_amdgcn_fence(__ATOMIC_ACQUIRE, "wavefront");
  };
  poll_kv(2, 0);
#pragma unroll
  for (int c = 2; c < 5; ++c) kv_copy(c);
  poll_kv(4, syn + 16);
  lds_wait_ge(syn + 56, 4);
#pragma unroll
  for (int c = 5; c < 9; ++c) kv_copy(c);
  if (wid == 0) {
    const int qa = max(tg * 64 - 2, 0) >> 8, qb = min(tg * 64 + 65, SEQ - 1) >> 8;
    const gu64* f = (const gu64*)(ws + WS_FB) + (lane < 32 ? qa : qb) * 32 + (lane & 31);
    const int bw = (lane & 7) * 8 + nb;
    const gu64* f2 = (const gu64*)(ws + WS_FB) + blk_qg(bw) * 32 + blk_head(bw);
    for (uint32_t spin = 0; spin < (1u << 16); ++spin) {
      const uint64_t v1 = __hip_atomic_load(f, __ATOMIC_RELAXED, __HIP_MEMORY_SCOPE_AGENT);
      const uint64_t v2 = __hip_atomic_load(f2, __ATOMIC_RELAXED, __HIP_MEMORY_SCOPE_AGENT);
      if (spin == 0) { wait_vm<0>(); lds_add_rtn(syn + 20); }
      if (__all(v1 == tag && v2 == tag)) break;
      __builtin_amdgcn_s_sleep(1);
    }
    lds_add_rtn(syn + 60);
  } else {
    wait_vm<0>();
    lds_add_rtn(syn + 20);
    lds_wait_ge(syn + 60, 1);
  }
  __builtin_amdgcn_fence(__ATOMIC_ACQUIRE, "wavefront");
  const int t0 = tg * 64;
  const int tm = wid & 1, tn = wid >> 1;
  auto ldf = [&](uint32_t voff, uint32_t soff) {
    return __builtin_bit_cast(bf16x8, __builtin_amdgcn_raw_buffer_load_b128(rws, voff, soff, 16));
  };
  auto ldc = [&](uint32_t voff, uint32_t soff) {
    return __builtin_bit_cast(bf16x8, __builtin_amdgcn_raw_buffer_load_b128(rws, voff, soff, 16));
  };
  {
    constexpr int D = 16;
    const uint32_t ho = (uint32_t)WS_H + (uint32_t)(hh * HTOK + t0 + tm * 32 + l31) * 16;
    const uint32_t wb = (uint32_t)WS_W2F + ((2 * nb + tn) * 32) * 1024;
    const uint32_t wo = lane * 16;
    bf16x8 fx[D], fw[D];
    auto issue = [&](int i) {
      const int g = i >> 3;
      const int sh = (g == 0) ? -1 : (g == 1) ? 1 : (g == 2) ? -2 : 2;
      fx[i % D] = ldf(ho, (uint32_t)(i * (2 * HTOK * 16) + 32 + sh * 16)) ;
      fw[i % D] = ldf(wo + (i & 3) * 1024, wb + (i >> 2) * 4096);
    };
#pragma unroll
    for (int i = 0; i < D; ++i) issue(i);
    const int tok = t0 + tm * 32 + l31;
    u32x4 xr[2];
#pragma unroll
    for (int s = 0; s < 2; ++s)
      xr[s] = *(const u32x4*)(x + (size_t)tok * DM + nb * 64 + tn * 32 + s * 16 + hh * 8);
    const u32x2 stu = __builtin_amdgcn_raw_buffer_load_b64(rws, tok * 16, (uint32_t)WS_ST, 16);
    __builtin_amdgcn_sched_barrier(0);
    f32x16 v = {};
#pragma unroll
    for (int i = 0; i < 32; ++i) {
      v = mfma32(fw[i % D], fx[i % D], v);
      __builtin_amdgcn_sched_barrier(0);
      if (i + D < 32) issue(i + D);
      if (i < 7) kv_copy(9 + i);
      __builtin_amdgcn_sched_barrier(0);
    }
    const float stt[2] = {u2f(stu[0]), u2f(stu[1])};
    float s1 = 0.f, s2 = 0.f;
#pragma unroll
    for (int s = 0; s < 2; ++s) {
      auto a = __builtin_amdgcn_permlane32_swap(xr[s][0], xr[s][2], false, false);
      auto c = __builtin_amdgcn_permlane32_swap(xr[s][1], xr[s][3], false, false);
      uint32_t d[4] = {a[0], c[0], a[1], c[1]};
#pragma unroll
      for (int j = 0; j < 4; ++j) {
        v[8 * s + 2 * j] += (bf_lo(d[j]) - stt[0]) * stt[1];
        v[8 * s + 2 * j + 1] += (bf_hi(d[j]) - stt[0]) * stt[1];
      }
    }
#pragma unroll
    for (int s = 0; s < 2; ++s) {
      u32x4 pk = pack8(v, s);
      st128l(pk, rws, (((nb * 4 + tn * 2 + s) * 2 + hh) * SEQ + tok) * 16, (uint32_t)WS_X1);
#pragma unroll
      for (int j = 0; j < 4; ++j) {
        s1 = dot2_ones(pk[j], s1);
        s2 = dot2_self(pk[j], s2);
      }
    }
    s1 += xor32(s1);
    s2 += xor32(s2);
    if (hh == 0)
      __builtin_amdgcn_raw_buffer_store_b64(u32x2{f2u(s1), f2u(s2)}, rws, ((nb * 2 + tn) * SEQ + tok) * 8, (uint32_t)WS_X1S, LAUX);
    const gu64* fb = (const gu64*)(ws + WS_FB) + lane;
    uint64_t fv[4];
#pragma unroll
    for (int j = 0; j < 4; ++j) fv[j] = __hip_atomic_load(fb + 64 * j, __ATOMIC_RELAXED, __HIP_MEMORY_SCOPE_AGENT);
    asm volatile("s_waitcnt vmcnt(0)" ::: "memory");
    lds_add_rtn(syn + 24);
    if (lds_add_rtn(syn + 4) == 3 && lane == 0)
      FLAGL(ws + WS_FX + tg * 128 + nb * 8, tag);
    for (uint32_t spin = 0; spin < (1u << 16); ++spin) {
      if (__all(fv[0] == tag && fv[1] == tag && fv[2] == tag && fv[3] == tag)) break;
      __builtin_amdgcn_s_sleep(1);
#pragma unroll
      for (int j = 0; j < 4; ++j) fv[j] = __hip_atomic_load(fb + 64 * j, __ATOMIC_RELAXED, __HIP_MEMORY_SCOPE_AGENT);
    }
    __builtin_amdgcn_fence(__ATOMIC_ACQUIRE, "wavefront");
    if (b == 0 && wid == 1 && lane == 0) __hip_atomic_store((gu64*)(ws + WS_EP), tag, __ATOMIC_RELAXED, __HIP_MEMORY_SCOPE_AGENT);
  }
  {
    constexpr int D = 5;
    lane = launder(lane);
    hh = lane >> 5, l31 = lane & 31;
    const int ng = tn, kh = tm;
    const int nt0 = nb < 3 ? 6 * nb : 18 + 5 * (nb - 3), ntl = nb < 3 ? nt0 + 5 : nt0 + 4;
    uint32_t wp[3];
#pragma unroll
    for (int u = 0; u < 3; ++u) wp[u] = (uint32_t)WS_W3F + (min(nt0 + ng * 3 + u, ntl) * 32 + kh * 16) * 1024;
    const uint32_t wo = lane * 16;
    auto ldw = [&](int i, int u) { return ldf(wo + (i & 3) * 1024, wp[u] + (i >> 2) * 4096); };
    bf16x8 fw[D][3], fx[D][2];
#pragma unroll
    for (int i = 0; i < D; ++i)
#pragma unroll
      for (int u = 0; u < 3; ++u) fw[i][u] = ldw(i, u);
    __builtin_amdgcn_sched_barrier(0);
    poll_flags(ws + WS_FX + tg * 128, 8, tag, syn + 8, wid, lane);
    const uint32_t xo0 = (uint32_t)WS_X1 + (uint32_t)((kh * 32 + hh) * SEQ + t0 + tm * 32 + l31) * 16;
    const uint32_t xo1 = xo0 + (tm ? -512 : 512);
    auto ldx = [&](int i) {
      fx[i % D][0] = ldc(xo0, i * (2 * SEQ * 16));
      fx[i % D][1] = ldc(xo1, i * (2 * SEQ * 16));
    };
#pragma unroll
    for (int i = 0; i < D; ++i) ldx(i);
    __builtin_amdgcn_sched_barrier(0);
    const uint32_t pv = ((lane & 15) == ((lane >> 4) & 1)) ? 0x3f803f80u : 0u;
    const bf16x8 pat = __builtin_bit_cast(bf16x8, u32x4{pv, pv, pv, pv});
    f32x16 acc[2][3] = {};
    f32x4 cs[3] = {};
    u32x2 sp[8];
    const int tok = t0 + tm * 32 + l31;
    __builtin_amdgcn_s_setprio(0);
#pragma unroll
    for (int i = 0; i < 16; ++i) {
      const int sl = i % D;
#pragma unroll
      for (int u = 0; u < 3; ++u) {
        acc[1][u] = mfma32(fw[sl][u], fx[sl][1], acc[1][u]);
        acc[0][u] = mfma32(fw[sl][u], fx[sl][0], acc[0][u]);
        cs[u] = mfma16(pat, fw[sl][u], cs[u]);
      }
      if (i + D < 16) {
        ldx(i + D);
#pragma unroll
        for (int u = 0; u < 3; ++u) fw[sl][u] = ldw(i + D, u);
      } else if (i + D < 16 + 4) {
#pragma unroll
        for (int j = 0; j < 2; ++j)
          sp[2 * (i + D - 16) + j] = __builtin_amdgcn_raw_buffer_load_b64(rws, (hh * 8 * SEQ + tok) * 8, (uint32_t)WS_X1S + (2 * (i + D - 16) + j) * SEQ * 8, 16);
      }
      __builtin_amdgcn_sched_barrier(0);
    }
    asm volatile("" : "+v"(acc[0][0]), "+v"(acc[0][1]), "+v"(acc[0][2]), "+v"(acc[1][0]), "+v"(acc[1][1]), "+v"(acc[1][2]));
    __builtin_amdgcn_s_setprio(3);
    lane = launder(lane);
    hh = lane >> 5, l31 = lane & 31;
    const int tok2 = t0 + tm * 32 + l31;
    {
      const uint32_t ca = lds_addr(smem) + K2_CS + ng * 384 + lane * 4;
      asm volatile(
          "s_mov_b64 exec, 0xffff\n\t"
          "ds_add_f32 %0, %1\n\tds_add_f32 %0, %2 offset:64\n\t"
          "ds_add_f32 %0, %3 offset:128\n\tds_add_f32 %0, %4 offset:192\n\t"
          "ds_add_f32 %0, %5 offset:256\n\tds_add_f32 %0, %6 offset:320\n\t"
          "s_mov_b64 exec, -1" ::"v"(ca), "v"(cs[0][0]), "v"(cs[0][1]), "v"(cs[1][0]), "v"(cs[1][1]), "v"(cs[2][0]),
          "v"(cs[2][1]) : "memory");
      lds_wait_ge(syn + 64, 4);
      char* dst = smem + (ng ? L_V : L_K) + kh * 12288 + lane * 16;
#pragma unroll
      for (int u = 0; u < 3; ++u)
#pragma unroll
        for (int q = 0; q < 4; ++q)
          *(f32x4*)(dst + u * 4096 + q * 1024) = f32x4{acc[1][u][4 * q], acc[1][u][4 * q + 1], acc[1][u][4 * q + 2], acc[1][u][4 * q + 3]};
      lds_add_rtn(syn + 32 + ng * 4);
    }
    float s1 = 0.f, s2 = 0.f;
#pragma unroll
    for (int i = 0; i < 8; ++i) { uint32_t a = sp[i][0], c = sp[i][1]; s1 += u2f(a); s2 += u2f(c); }
    s1 += xor32(s1);
    s2 += xor32(s2);
    const float mean = s1 * (1.0f / DM);
    const float var = fmaxf(s2 * (1.0f / DM) - mean * mean, 0.f);
    const float rs = __builtin_amdgcn_rsqf(var + LN_EPS);
    const f32x2 nm2 = {-mean, -mean}, rs2 = {rs, rs};
    const float* c0 = (const float*)(smem + K2_CS) + ng * 96 + 4 * hh;
    lds_wait_ge(syn + 32 + ng * 4, 2);
    const char* src = smem + (ng ? L_V : L_K) + (kh ^ 1) * 12288 + lane * 16;
#pragma unroll
    for (int u = 0; u < 3; ++u) {
      const int nt = nt0 + ng * 3 + u;
      if (nt <= ntl) {
        const f32x16& a = acc[0][u];
#pragma unroll
        for (int s = 0; s < 2; ++s) {
          u32x4 pk;
#pragma unroll
          for (int q = 0; q < 2; ++q) {
            const f32x4 c = *(const f32x4*)(c0 + u * 32 + 8 * (2 * s + q));
            const f32x4 o = *(const f32x4*)(src + u * 4096 + (2 * s + q) * 1024);
#pragma unroll
            for (int j = 0; j < 2; ++j) {
              const int r = 8 * s + 4 * q + 2 * j;
              f32x2 z = __builtin_elementwise_fma(nm2, f32x2{c[2 * j], c[2 * j + 1]}, f32x2{a[r], a[r + 1]} + f32x2{o[2 * j], o[2 * j + 1]}) * rs2;
              z = gelu2(z);
              pk[2 * q + j] = pk2(z[0], z[1]);
            }
          }
          st128l(pk, rws, (hh * SEQ + tok2) * 16, (uint32_t)WS_OG + (32 + nt * 2 + s) * (2 * SEQ * 16));
        }
      }
    }
    asm volatile("s_waitcnt vmcnt(0)" ::: "memory");
    if (lds_add_rtn(syn + 12) == 3 && lane == 0)
      FLAGL(ws + WS_FG + tg * 128 + nb * 8, tag);
  }
  {
    constexpr int D = 8, NG = 22;
    constexpr int JF = 12, JC = 18;
    lane = launder(lane);
    hh = lane >> 5, l31 = lane & 31;
    const uint32_t wo = lane * 16;
    const uint32_t wb = (uint32_t)WS_W4F + ((2 * nb + tn) * KOG + wid) * 1024;
    const uint32_t wb1 = (uint32_t)WS_W4F + ((2 * nb + (tn ^ 1)) * KOG + wid) * 1024;
    const uint32_t xo = (uint32_t)WS_OG + (uint32_t)((wid * 2 + hh) * SEQ + t0 + tm * 32 + l31) * 16;
    const uint32_t xo1 = (uint32_t)WS_OG + (uint32_t)((wid * 2 + hh) * SEQ + t0 + (tm ^ 1) * 32 + l31) * 16;
    const int last = wid < 2 ? NG - 1 : NG - 2;
    auto k16 = [&](int j) { return j < NG ? 32 + 4 * (j == NG - 1 ? last : j) : 4 * (j - NG); };
    bf16x8 fw[D][2], fx[D][2];
    auto ldw = [&](int j) {
      fw[j % D][0] = ldf(wo, wb + k16(j) * 1024);
      fw[j % D][1] = ldf(wo, wb1 + k16(j) * 1024);
    };
    auto ldx = [&](int j) {
      fx[j % D][0] = ldc(xo, k16(j) * (2 * SEQ * 16));
      fx[j % D][1] = ldc(xo1, k16(j) * (2 * SEQ * 16));
    };
#pragma unroll
    for (int j = 0; j < D; ++j) ldw(j);
    __builtin_amdgcn_sched_barrier(0);
    poll_flags(ws + WS_FG + tg * 128, 8, tag, syn + 16 + 24, wid, lane);
#pragma unroll
    for (int j = 0; j < D; ++j) ldx(j);
    __builtin_amdgcn_sched_barrier(0);
    f32x16 acc[2][2] = {};
    const uint32_t rag = wid < 2 ? 0xffffffffu : 0u;
    auto step = [&](int j) {
      if (j == NG - 1) {
#pragma unroll
        for (int a = 0; a < 2; ++a) fx[j % D][a] = __builtin_bit_cast(bf16x8, __builtin_bit_cast(u32x4, fx[j % D][a]) & u32x4{rag, rag, rag, rag});
      }
#pragma unroll
      for (int a = 0; a < 2; ++a)
#pragma unroll
        for (int t = 0; t < 2; ++t) acc[1 - a][1 - t] = mfma32(fw[j % D][1 - t], fx[j % D][1 - a], acc[1 - a][1 - t]);
    };
    const gu64* fo = (const gu64*)(ws + WS_FO + (tg >> 2) * 256) + (lane & 31);
    uint64_t fv = 0;
    __builtin_amdgcn_s_setprio(0);
#pragma unroll
    for (int j = 0; j < JC; ++j) {
      step(j);
      if (j + D < NG) ldx(j + D);
      ldw(j + D);
      if (j == JF) fv = __hip_atomic_load(fo, __ATOMIC_RELAXED, __HIP_MEMORY_SCOPE_AGENT);
      __builtin_amdgcn_sched_barrier(0);
    }
    const int tok = t0 + tm * 32 + l31;
    u32x4 xr[2], x1r[2];
    auto ld_epi = [&]() {
#pragma unroll
      for (int s = 0; s < 2; ++s) {
        const int n16 = nb * 4 + tn * 2 + s;
        xr[s] = *(const u32x4*)(x + (size_t)tok * DM + n16 * 16 + hh * 8);
        x1r[s] = __builtin_bit_cast(u32x4, ldc((hh * SEQ + tok) * 16, (uint32_t)WS_X1 + n16 * (2 * SEQ * 16)));
      }
    };
    const bool rdy = __all(fv == tag);
    if (rdy) {
      __builtin_amdgcn_fence(__ATOMIC_ACQUIRE, "wavefront");
#pragma unroll
      for (int j = NG; j < JC + D; ++j) ldx(j);
    }
    __builtin_amdgcn_sched_barrier(0);
#pragma unroll
    for (int j = JC; j < NG; ++j) {
      step(j);
      if (rdy) ldx(j + D);
      ldw(j + D);
      __builtin_amdgcn_sched_barrier(0);
    }
    __builtin_amdgcn_s_setprio(3);
    ld_epi();
    if (!rdy) {
      for (uint32_t spin = 0; spin < (1u << 16); ++spin) {
        if (__all(__hip_atomic_load(fo, __ATOMIC_RELAXED, __HIP_MEMORY_SCOPE_AGENT) == tag)) break;
        __builtin_amdgcn_s_sleep(1);
      }
      __builtin_amdgcn_fence(__ATOMIC_ACQUIRE, "wavefront");
#pragma unroll
      for (int j = NG; j < NG + 8; ++j) ldx(j);
    }
    __builtin_amdgcn_sched_barrier(0);
#pragma unroll
    for (int j = NG; j < NG + 8; ++j) step(j);
    lds_wait_ge(syn + 28, 4);
#pragma unroll
    for (int d = 1; d < 4; ++d) {
      const f32x16& a = acc[d & 1][d >> 1];
      char* dst = smem + ((wid ^ d) * 3 + d - 1) * 4096 + lane * 16;
#pragma unroll
      for (int q = 0; q < 4; ++q) *(f32x4*)(dst + q * 1024) = f32x4{a[4 * q], a[4 * q + 1], a[4 * q + 2], a[4 * q + 3]};
    }
    role_bar(syn + 16 + 32);
    f32x4 sm[4];
#pragma unroll
    for (int q = 0; q < 4; ++q) sm[q] = f32x4{acc[0][0][4 * q], acc[0][0][4 * q + 1], acc[0][0][4 * q + 2], acc[0][0][4 * q + 3]};
#pragma unroll
    for (int w = 0; w < 3; ++w)
#pragma unroll
      for (int q = 0; q < 4; ++q) sm[q] += *(const f32x4*)(smem + (wid * 3 + w) * 4096 + q * 1024 + lane * 16);
#pragma unroll
    for (int s = 0; s < 2; ++s) {
      float v[8], w8[8];
#pragma unroll
      for (int j = 0; j < 2; ++j) {
        v[2 * j] = sm[2 * s][2 * j] + bf_lo(x1r[s][j]);
        v[2 * j + 1] = sm[2 * s][2 * j + 1] + bf_hi(x1r[s][j]);
        v[4 + 2 * j] = sm[2 * s + 1][2 * j] + bf_lo(x1r[s][2 + j]);
        v[4 + 2 * j + 1] = sm[2 * s + 1][2 * j + 1] + bf_hi(x1r[s][2 + j]);
      }
#pragma unroll
      for (int j = 0; j < 4; ++j) swap32(v[j], v[4 + j], w8[j], w8[4 + j]);
      u32x4 o;
#pragma unroll
      for (int j = 0; j < 4; ++j) o[j] = pk2(w8[2 * j] + bf_lo(xr[s][j]), w8[2 * j + 1] + bf_hi(xr[s][j]));
      *(u32x4*)(out + (size_t)tok * DM + (nb * 4 + tn * 2 + s) * 16 + hh * 8) = o;
    }
  }
}


__global__ __launch_bounds__(512, 2) void fused_kernel(const bf16_t* __restrict__ x, const bf16_t* __restrict__ wqkv,
                                                       const bf16_t* __restrict__ w1, const bf16_t* __restrict__ w2,
                                                       const bf16_t* __restrict__ w3, const bf16_t* __restrict__ wo,
                                                       const bf16_t* __restrict__ w4, char* __restrict__ ws,
                                                       bf16_t* __restrict__ out) {
  extern __shared__ __attribute__((aligned(16))) char smem[];
  const int tid = threadIdx.x, lane = tid & 63;
  const int wid = __builtin_amdgcn_readfirstlane(tid >> 6);
  const int b = blockIdx.x;
  const int head = blk_head(b), qg = blk_qg(b);
  const __amdgpu_buffer_rsrc_t rws = __builtin_amdgcn_make_buffer_rsrc(ws, 0, (int)WS_END, 0x00020000);
  asm volatile("" ::"s"(x), "s"(wqkv), "s"(w1), "s"(w2), "s"(w3), "s"(wo), "s"(w4), "s"(ws), "s"(out));
  if (tid < 224) *(uint32_t*)(smem + K2_SYN + tid * 4) = 0u;
  const uint64_t tag = phase1(smem, x, wqkv, w1, w2, w3, wo, w4, ws, out, rws, b, head, qg, wid, lane);
  if (wid < 4) {
    attention_wave(smem, ws, rws, tag, head, qg * 8 + wid * 2, wid, lane, lds_addr(smem) + K2_CS + 1536 + wid * 128);
  } else {
    __builtin_amdgcn_s_setprio(3);
    mixer_wave(smem, x, ws, out, rws, tag, b, head, qg, wid - 4, lane);
  }
  asm volatile(".Lpf_end:");
}
extern "C" void kernel_launch(void* const* d_in, const int* in_sizes, int n_in, void* d_out, int out_size, void* d_ws,
                              size_t ws_size, hipStream_t stream) {
  const bf16_t* x = (const bf16_t*)d_in[0];
  const bf16_t* wqkv = (const bf16_t*)d_in[1];
  const bf16_t* wo = (const bf16_t*)d_in[2];
  const bf16_t* w1 = (const bf16_t*)d_in[4];
  const bf16_t* w2 = (const bf16_t*)d_in[5];
  const bf16_t* w3 = (const bf16_t*)d_in[6];
  const bf16_t* w4 = (const bf16_t*)d_in[7];
  (void)hipFuncSetAttribute((const void*)fused_kernel, hipFuncAttributeMaxDynamicSharedMemorySize, K2_LDS);
  fused_kernel<<<256, 512, K2_LDS, stream>>>(x, wqkv, w1, w2, w3, wo, w4, (char*)d_ws, (bf16_t*)d_out);
}
```
